```python
import torch

def _softmax(x, axis=-1):
    return torch.softmax(x, dim=axis)
def _gelu(x):
    return torch.nn.functional.gelu(x, approximate="tanh")
def _silu(x):
    return torch.nn.functional.silu(x)
def _sigmoid(x):
    return torch.sigmoid(x)
def _layernorm(x, axis=-1, eps=1e-5):
    m = x.mean(dim=axis, keepdim=True)
    v = x.var(dim=axis, keepdim=True, unbiased=False)
    return (x - m) / torch.sqrt(v + eps)
def _scatter_add(tgt, idx, src, axis=0):
    return tgt.clone().index_add_(axis, idx, src)
def _segment_sum(src, seg, n):
    out = torch.zeros((n,) + src.shape[1:], dtype=src.dtype)
    return out.index_add_(0, seg, src)
def _pad(x, pw):
    flat = tuple(v for pair in reversed(pw) for v in pair)
    return torch.nn.functional.pad(x, flat)

kernel_name = 'sequential_preLN__linear_attn_block__glu_gated_bilinear__prompt'

def setup_inputs():

    _w = (lambda a: (lambda t: t.to(torch.bfloat16) if t.is_floating_point() else t)(torch.as_tensor(a)))
    def _A_setup():
        B, S, D = 1, 2048, 448
        return {
            "x": _w((lambda *s: torch.randn(*s, dtype=torch.bfloat16) * 0.05)(B, S, D)),
            "wqkv": _w((lambda *s: torch.randn(*s, dtype=torch.bfloat16) * 0.1)(D, 3 * D)),
            "wo": _w((lambda *s: torch.randn(*s, dtype=torch.bfloat16) * 0.05)(D, D)),
        }
    _A_in = _A_setup()

    def _B_setup():
        B, S, D, F = 1, 2048, 448, 1792
        return {
            "x": _w((lambda *s: torch.randn(*s, dtype=torch.bfloat16) * 0.05)(B, S, D)),
            "w_gate": _w((lambda *s: torch.randn(*s, dtype=torch.bfloat16) * 0.4)(D, F) * (D ** -0.5)),
            "w_up": _w((lambda *s: torch.randn(*s, dtype=torch.bfloat16) * 0.4)(D, F) * (D ** -0.5)),
            "w_down": _w((lambda *s: torch.randn(*s, dtype=torch.bfloat16) * 0.4)(F, D) * (F ** -0.5)),
        }
    _B_in = _B_setup()

    return {
        "A_x": _A_in["x"],
        "A_wqkv": _A_in["wqkv"],
        "A_wo": _A_in["wo"],
        "B_x": _B_in["x"],
        "B_w_gate": _B_in["w_gate"],
        "B_w_up": _B_in["w_up"],
        "B_w_down": _B_in["w_down"],
    }

def reference(A_x, A_wqkv, A_wo, B_x, B_w_gate, B_w_up, B_w_down):
    def _A_ref(x, wqkv, wo):
        B, S, D = x.shape
        H = 16; Dh = D // H
        qkv = (lambda x, *d: x.permute(*d))((x @ wqkv).reshape(B, S, 3, H, Dh), 2, 0, 3, 1, 4)
        q, k, v = qkv[0], qkv[1], qkv[2]
        phi = lambda t: torch.where(t > 0, t + 1, torch.exp(t)) + 1e-6
        pq, pk = phi(q), phi(k)
        kv = torch.einsum("bhsd,bhse->bhde", pk, v)
        z = torch.einsum("bhsd->bhd", pk)
        num = torch.einsum("bhqd,bhde->bhqe", pq, kv)
        den = torch.einsum("bhqd,bhd->bhq", pq, z)[..., None] + 1e-6
        o = num / den
        return (lambda x, *d: x.permute(*d))(o, 0, 2, 1, 3).reshape(B, S, D) @ wo

    def _B_ref(x, w_gate, w_up, w_down):
        g = torch.einsum("bsd,df->bsf", x, w_gate)
        u = torch.einsum("bsd,df->bsf", x, w_up)
        h = g * u
        return torch.einsum("bsf,fd->bsd", h, w_down)

    x = A_x
    h = x + _A_ref(_layernorm(x, axis=-1), A_wqkv, A_wo)
    h2 = h + _B_ref(_layernorm(h, axis=-1), B_w_gate, B_w_up, B_w_down)
    return h2
```

```cpp
#include <hip/hip_runtime.h>
#include <hip/hip_bf16.h>
typedef unsigned short u16;
constexpr int S=2048, D=448, F=1792, H=16, DH=28;
__device__ inline float b2f(u16 v){ return __builtin_bit_cast(float, (unsigned)v<<16); }
__device__ inline u16 f2b(float f){ unsigned u=__builtin_bit_cast(unsigned,f); u += 0x7fff + ((u>>16)&1); return u>>16; }
__global__ void ln_k(const u16* x, u16* y){
  int r=blockIdx.x; int t=threadIdx.x;
  float s=0, ss=0; float v[7];
  for(int i=0;i<7;i++){ v[i]=b2f(x[r*D+t+64*i]); s+=v[i]; }
  for(int o=32;o;o>>=1) s+=__shfl_xor(s,o);
  float m=s/D;
  for(int i=0;i<7;i++){ float d=v[i]-m; ss+=d*d; }
  for(int o=32;o;o>>=1) ss+=__shfl_xor(ss,o);
  float rs=rsqrtf(ss/D+1e-5f);
  for(int i=0;i<7;i++) y[r*D+t+64*i]=f2b((v[i]-m)*rs);
}
__global__ void gemm_k(const u16* A, const u16* B, u16* C, const u16* res, int N, int K, int mode){
  int n=blockIdx.x*64+threadIdx.x; int m=blockIdx.y;
  float acc=0;
  for(int k=0;k<K;k++) acc+=b2f(A[m*K+k])*b2f(B[k*N+n]);
  if(mode==1 && n<896){ acc = (acc>0? acc+1.f : __expf(acc)) + 1e-6f; }
  if(mode==2) acc+=b2f(res[m*N+n]);
  C[m*N+n]=f2b(acc);
}
__global__ void kv_k(const u16* qkv, float* kv){
  int h=blockIdx.x/DH, d=blockIdx.x%DH, e=threadIdx.x;
  if(e>28) return;
  float acc=0;
  for(int s=0;s<S;s++){ float pk=b2f(qkv[s*1344+448+h*DH+d]); float v = e<28? b2f(qkv[s*1344+896+h*DH+e]) : 1.f; acc+=pk*v; }
  kv[(h*DH+d)*32+e]=acc;
}
__global__ void o_k(const u16* qkv, const float* kv, u16* o){
  int s=blockIdx.x; int c=threadIdx.x; if(c>=D) return; int h=c/DH, e=c%DH;
  float num=0, den=0;
  for(int d=0;d<DH;d++){ float pq=b2f(qkv[s*1344+h*DH+d]); num+=pq*kv[(h*DH+d)*32+e]; den+=pq*kv[(h*DH+d)*32+28]; }
  o[s*D+c]=f2b(num/(den+1e-6f));
}
__global__ void mul_k(const u16* g, const u16* u, u16* h){ int i=blockIdx.x*256+threadIdx.x; h[i]=f2b(b2f(g[i])*b2f(u[i])); }
extern "C" void kernel_launch(void* const* d_in, const int* in_sizes, int n_in, void* d_out, int out_size, void* d_ws, size_t ws_size, hipStream_t stream){
  const u16* x=(const u16*)d_in[0]; const u16* wqkv=(const u16*)d_in[1]; const u16* wo=(const u16*)d_in[2];
  const u16* wg=(const u16*)d_in[4]; const u16* wu=(const u16*)d_in[5]; const u16* wd=(const u16*)d_in[6];
  char* ws=(char*)d_ws;
  u16* xn=(u16*)ws; u16* qkv=(u16*)(ws+(4<<20)); float* kv=(float*)(ws+(12<<20)); u16* o=(u16*)(ws+(16<<20));
  u16* h=(u16*)(ws+(20<<20)); u16* hn=(u16*)(ws+(24<<20)); u16* g=(u16*)(ws+(28<<20)); u16* u=(u16*)(ws+(40<<20)); u16* hh=(u16*)(ws+(52<<20));
  ln_k<<<S,64,0,stream>>>(x,xn);
  gemm_k<<<dim3(1344/64,S),64,0,stream>>>(xn,wqkv,qkv,nullptr,1344,D,1);
  kv_k<<<H*DH,32,0,stream>>>(qkv,kv);
  o_k<<<S,448,0,stream>>>(qkv,kv,o);
  gemm_k<<<dim3(D/64,S),64,0,stream>>>(o,wo,h,x,D,D,2);
  ln_k<<<S,64,0,stream>>>(h,hn);
  gemm_k<<<dim3(F/64,S),64,0,stream>>>(hn,wg,g,nullptr,F,D,0);
  gemm_k<<<dim3(F/64,S),64,0,stream>>>(hn,wu,u,nullptr,F,D,0);
  mul_k<<<S*F/256,256,0,stream>>>(g,u,hh);
  gemm_k<<<dim3(D/64,S),64,0,stream>>>(hh,wd,(u16*)d_out,h,D,F,2);
}
```

```cpp
#include <hip/hip_runtime.h>
#include <hip/hip_bf16.h>

__global__ __launch_bounds__(256, 1) void fused_block_kernel(const void* x, const void* wqkv, const void* wo, const void* wg,
                                                             const void* wu, const void* wd, void* out, void* ws) {
  unsigned tid = threadIdx.x;
  unsigned bid = blockIdx.x;
  asm volatile(
"s_load_dwordx2 s[4:5], %1, 0x0\n"
"s_load_dwordx2 s[8:9], %1, 0x8\n"
"s_load_dwordx2 s[24:25], %1, 0x38\n"
"s_load_dwordx2 s[12:13], %1, 0x10\n"
"s_load_dwordx2 s[20:21], %1, 0x28\n"
"s_load_dwordx2 s[28:29], %1, 0x30\n"
"s_mov_b32 s87, %2\n"
"v_lshrrev_b32 v2, 6, %0\n"
"v_and_b32 v1, 63, %0\n"
"v_readfirstlane_b32 s68, v2\n"
"s_lshr_b32 s74, s87, 3\n"
"s_getpc_b64 s[34:35]\n"
"L_pc_%=:\n"
"s_lshl_b32 s36, s74, 11\n"
"v_lshl_add_u32 v4, %0, 3, s36\n"
"v_min_u32 v4, L_codeend_%= - L_pc_%= - 8, v4\n"
"global_load_dwordx2 v[6:7], v4, s[34:35]\n"
"s_and_b32 s73, s87, 6\n"
"s_lshr_b32 s75, s74, 3\n"
"s_lshl_b32 s69, s73, 1\n"
"s_add_u32 s69, s69, s75\n"
"s_and_b32 s71, s87, 1\n"
"s_and_b32 s72, s74, 7\n"
"s_lshl_b32 s70, s71, 3\n"
"s_add_u32 s70, s70, s72\n"
"s_lshr_b32 s82, s68, 1\n"
"s_lshl_b32 s73, s82, 3\n"
"s_add_u32 s73, s73, 24\n"
"s_load_dwordx2 s[16:17], %1, s73\n"
"s_lshl_b32 s80, s69, 2\n"
"s_add_u32 s80, s80, s68\n"
"s_mov_b32 s6, 1835008\n"
"s_mov_b32 s7, 0x20000\n"
"s_mov_b32 s10, 1204224\n"
"s_mov_b32 s11, 0x20000\n"
"v_lshrrev_b32 v140, 3, v1\n"
"s_lshl_b32 s73, s80, 5\n"
"v_add_u32 v2, s73, v140\n"
"v_mul_u32_u24 v2, 896, v2\n"
"v_and_b32 v141, 7, v1\n"
"v_lshrrev_b32 v3, 4, v1\n"
"v_xor_b32 v141, v141, v3\n"
"v_lshl_add_u32 v112, v141, 4, v2\n"
"s_movk_i32 s88, 6144\n"
"s_movk_i32 s89, 12288\n"
"s_movk_i32 s90, 18432\n"
"s_lshl_b32 s85, s68, 12\n"
"s_lshl_b32 s43, s68, 10\n"
"s_mul_i32 s73, s70, 56\n"
"s_cmp_eq_u32 s70, 15\n"
"s_cselect_b32 s74, 8, 0\n"
"s_sub_u32 s73, s73, s74\n"
"s_mul_i32 s75, s68, 43008\n"
"s_add_u32 s73, s73, s75\n"
"s_add_u32 s73, s73, 896\n"
"v_lshrrev_b32 v2, 2, v1\n"
"v_mul_u32_u24 v2, 2688, v2\n"
"v_and_b32 v3, 3, v1\n"
"v_lshlrev_b32 v3, 4, v3\n"
"v_add3_u32 v113, v2, v3, s73\n"
"s_and_b32 s73, s69, 3\n"
"s_add_u32 s73, s73, s72\n"
"s_lshl_b32 s73, s73, 2\n"
"s_mov_b32 vcc_lo, 0x30530530\n"
"s_mov_b32 vcc_hi, 0x305305\n"
"s_lshr_b64 vcc, vcc, s73\n"
"s_and_b32 s73, vcc_lo, 15\n"
"s_mul_i32 s78, s73, 172032\n"
"v_lshl_add_u32 v112, s73, 7, v112\n"
"s_sub_u32 s45, s72, s73\n"
"s_cselect_b32 s75, 7, 0\n"
"s_add_u32 s45, s45, s75\n"
"s_cmp_eq_u32 s72, 7\n"
"s_cselect_b32 s45, 7, s45\n"
"s_add_u32 s50, s78, 172032\n"
"s_cmp_eq_u32 s50, 1204224\n"
"s_cselect_b32 s50, 0, s50\n"
"s_add_u32 s35, s50, 172032\n"
"s_cmp_eq_u32 s35, 1204224\n"
"s_cselect_b32 s35, 0, s35\n"
"s_movk_i32 s42, 128\n"
"v_xor_b32 v115, 64, v112\n"
"v_lshlrev_b32 v132, 4, v1\n"
"s_and_b32 s83, s68, 1\n"
"s_mov_b32 s77, 0x3f803f80\n"
"v_mov_b32 v64, s77\n"
"v_mov_b32 v65, s77\n"
"v_mov_b32 v66, s77\n"
"v_mov_b32 v67, s77\n"
"s_lshr_b32 s75, s85, 1\n"
"v_add_u32 v69, s75, v132\n"
"s_waitcnt lgkmcnt(0)\n"
"s_mov_b32 m0, s85\n"
"s_nop 0\n"
"buffer_load_dwordx4 v112, s[4:7], 0 offen offset:0 lds\n"
"buffer_load_dwordx4 v115, s[4:7], s88 offen offset:1024 lds\n"
"buffer_load_dwordx4 v112, s[4:7], s89 offen offset:2048 lds\n"
"buffer_load_dwordx4 v115, s[4:7], s90 offen offset:3072 lds\n"
"s_add_u32 m0, s43, 131072\n"
"s_nop 0\n"
"buffer_load_dwordx4 v113, s[8:11], s78 offen offset:0 lds\n"
"ds_write_b128 v69, v[64:67] offset:49152\n"
"ds_write_b128 v69, v[64:67] offset:50176\n"
"s_xor_b32 s91, s68, 0\n"
"s_lshl_b32 s91, s91, 10\n"
"s_xor_b32 s92, s68, 1\n"
"s_lshl_b32 s92, s92, 10\n"
"s_xor_b32 s93, s68, 2\n"
"s_lshl_b32 s93, s93, 10\n"
"s_cmp_lg_u32 s50, 0\n"
"s_cselect_b32 s41, s42, 0xfffffd00\n"
"v_add_u32 v112, s41, v112\n"
"v_add_u32 v115, s41, v115\n"
"s_add_u32 m0, s85, 16384\n"
"s_nop 0\n"
"buffer_load_dwordx4 v112, s[4:7], 0 offen offset:0 lds\n"
"s_xor_b32 s94, s68, 3\n"
"s_lshl_b32 s94, s94, 10\n"
"v_and_b32 v2, 31, v1\n"
"v_lshlrev_b32 v130, 2, v2\n"
"v_lshlrev_b32 v2, 7, v2\n"
"v_bfe_u32 v3, v1, 1, 3\n"
"v_lshrrev_b32 v140, 5, v1\n"
"v_xor_b32 v3, v3, v140\n"
"buffer_load_dwordx4 v115, s[4:7], s88 offen offset:1024 lds\n"
"s_lshr_b32 s75, s91, 9\n"
"v_xor_b32 v141, s75, v3\n"
"v_lshl_add_u32 v141, v141, 4, v2\n"
"v_add_u32 v116, s85, v141\n"
"s_lshr_b32 s75, s92, 9\n"
"v_xor_b32 v141, s75, v3\n"
"v_lshl_add_u32 v141, v141, 4, v2\n"
"v_add_u32 v117, s85, v141\n"
"buffer_load_dwordx4 v112, s[4:7], s89 offen offset:2048 lds\n"
"s_lshr_b32 s75, s93, 9\n"
"v_xor_b32 v141, s75, v3\n"
"v_lshl_add_u32 v141, v141, 4, v2\n"
"v_add_u32 v118, s85, v141\n"
"s_lshr_b32 s75, s94, 9\n"
"v_xor_b32 v141, s75, v3\n"
"v_lshl_add_u32 v141, v141, 4, v2\n"
"v_add_u32 v119, s85, v141\n"
"buffer_load_dwordx4 v115, s[4:7], s90 offen offset:3072 lds\n"
"s_lshl_b32 s75, s71, 13\n"
"s_lshl_b32 s76, s83, 12\n"
"s_add_u32 s75, s75, s76\n"
"s_lshl_b32 s76, s82, 2\n"
"v_xor_b32 v3, s76, v3\n"
"v_xor_b32 v141, 0, v3\n"
"v_lshl_add_u32 v141, v141, 4, v2\n"
"v_add_u32 v120, s75, v141\n"
"s_add_u32 m0, s43, 135168\n"
"s_nop 0\n"
"buffer_load_dwordx4 v113, s[8:11], s50 offen offset:0 lds\n"
"v_xor_b32 v141, 2, v3\n"
"v_lshl_add_u32 v141, v141, 4, v2\n"
"v_add_u32 v121, s75, v141\n"
"s_lshr_b32 s74, s74, 3\n"
"v_bfe_u32 v2, v1, 2, 2\n"
"v_lshl_add_u32 v2, v140, 3, v2\n"
"v_lshlrev_b32 v2, 6, v2\n"
"v_bfe_u32 v141, v1, 4, 1\n"
"s_cmp_lg_u32 s35, 0\n"
"s_cselect_b32 s41, s42, 0xfffffd00\n"
"v_add_u32 v112, s41, v112\n"
"v_add_u32 v115, s41, v115\n"
"s_add_u32 m0, s85, 32768\n"
"s_nop 0\n"
"buffer_load_dwordx4 v112, s[4:7], 0 offen offset:0 lds\n"
"v_and_b32 v3, 3, v1\n"
"v_lshl_add_u32 v3, v141, 2, v3\n"
"v_cmp_eq_u32 vcc, 7, v3\n"
"v_add_u32 v3, s74, v3\n"
"v_and_b32 v3, 7, v3\n"
"v_lshl_add_u32 v123, v3, 3, v2\n"
"v_mov_b32 v124, 131072\n"
"v_mov_b32 v3, 49152\n"
"buffer_load_dwordx4 v115, s[4:7], s88 offen offset:1024 lds\n"
"v_cndmask_b32 v3, v124, v3, vcc\n"
"v_add_u32 v3, v3, v123\n"
"v_mov_b32 v122, 8192\n"
"v_cndmask_b32 v122, v122, 0, vcc\n"
"v_add_u32 v125, s91, v3\n"
"v_add_u32 v126, s92, v3\n"
"v_add_u32 v127, s93, v3\n"
"v_add_u32 v128, s94, v3\n"
"buffer_load_dwordx4 v112, s[4:7], s89 offen offset:2048 lds\n"
"v_accvgpr_write_b32 a116, v64\n"
"v_accvgpr_write_b32 a117, v64\n"
"v_accvgpr_write_b32 a118, v64\n"
"v_accvgpr_write_b32 a119, v64\n"
"s_add_u32 s79, s35, 172032\n"
"s_cmp_eq_u32 s79, 1204224\n"
"s_cselect_b32 s79, 0, s79\n"
"s_add_u32 s95, s79, 172032\n"
"s_cmp_eq_u32 s95, 1204224\n"
"s_cselect_b32 s95, 0, s95\n"
"s_add_u32 s84, s95, 172032\n"
"s_cmp_eq_u32 s84, 1204224\n"
"s_cselect_b32 s84, 0, s84\n"
"s_add_u32 s86, s84, 172032\n"
"s_cmp_eq_u32 s86, 1204224\n"
"s_cselect_b32 s86, 0, s86\n"
"buffer_load_dwordx4 v115, s[4:7], s90 offen offset:3072 lds\n"
"v_add_u32 v69, 147456, v69\n"
"v_accvgpr_write_b32 a126, v69\n"
"v_and_b32 v3, 1, v1\n"
"v_lshl_add_u32 v141, v141, 1, v3\n"
"v_bfe_u32 v3, v1, 1, 1\n"
"v_lshl_add_u32 v3, v141, 1, v3\n"
"v_add_u32 v3, s74, v3\n"
"s_add_u32 m0, s43, 139264\n"
"s_nop 0\n"
"buffer_load_dwordx4 v113, s[8:11], s35 offen offset:0 lds\n"
"v_and_b32 v3, 7, v3\n"
"v_lshl_add_u32 v124, v3, 3, v2\n"
"s_mov_b32 s26, 17825792\n"
"s_mov_b32 s27, 0x20000\n"
"s_lshl_b32 s34, s70, 15\n"
"s_lshl_b32 s75, s83, 10\n"
"s_add_u32 s34, s34, s75\n"
"s_lshl_b32 s75, s82, 9\n"
"s_add_u32 s34, s34, s75\n"
"s_add_u32 s34, s34, 262144\n"
"s_waitcnt vmcnt(10)\n"
"s_waitcnt lgkmcnt(0)\n"
"s_barrier\n"
"s_cmp_eq_u32 s45, 0\n"
"s_cbranch_scc1 L_res_1_%=\n"
"L_resb_2_%=:\n"
"ds_read_b128 v[144:147], v116 offset:0\n"
"ds_read_b64_tr_b16 v[80:81], v125 offset:0\n"
"ds_read_b64_tr_b16 v[82:83], v125 offset:256\n"
"ds_read_b128 v[148:151], v117 offset:0\n"
"ds_read_b64_tr_b16 v[84:85], v126 offset:0\n"
"ds_read_b64_tr_b16 v[86:87], v126 offset:256\n"
"ds_read_b128 v[152:155], v118 offset:0\n"
"ds_read_b64_tr_b16 v[88:89], v127 offset:0\n"
"ds_read_b64_tr_b16 v[90:91], v127 offset:256\n"
"ds_read_b128 v[156:159], v119 offset:0\n"
"ds_read_b64_tr_b16 v[92:93], v128 offset:0\n"
"ds_read_b64_tr_b16 v[94:95], v128 offset:256\n"
"s_cmp_lg_u32 s79, 0\n"
"s_cselect_b32 s41, s42, 0xfffffd00\n"
"v_add_u32 v112, s41, v112\n"
"v_add_u32 v115, s41, v115\n"
"s_add_u32 m0, s85, 65536\n"
"s_nop 0\n"
"buffer_load_dwordx4 v112, s[4:7], 0 offen offset:0 lds\n"
"s_lshl_b32 s36, s69, 11\n"
"s_add_u32 s36, s36, s34\n"
"s_mov_b32 s52, -1\n"
"s_mov_b32 s53, 0\n"
"buffer_load_dwordx4 v115, s[4:7], s88 offen offset:1024 lds\n"
"v_accvgpr_write_b32 a204, -1\n"
"v_accvgpr_write_b32 a205, -1\n"
"v_accvgpr_write_b32 a206, -1\n"
"v_accvgpr_write_b32 a207, -1\n"
"buffer_load_dwordx4 v112, s[4:7], s89 offen offset:2048 lds\n"
"s_lshl_b32 s37, s80, 15\n"
"s_lshl_b32 s75, s70, 11\n"
"s_add_u32 s37, s37, s75\n"
"buffer_load_dwordx4 v115, s[4:7], s90 offen offset:3072 lds\n"
"s_add_u32 s37, s37, 786432\n"
"s_lshl_b32 s81, s69, 2\n"
"s_lshl_b32 s75, s71, 1\n"
"s_add_u32 m0, s43, 143360\n"
"s_nop 0\n"
"buffer_load_dwordx4 v113, s[8:11], s79 offen offset:0 lds\n"
"s_add_u32 s81, s81, s75\n"
"s_add_u32 s81, s81, s83\n"
"s_mul_i32 s38, s81, 28672\n"
"s_lshl_b32 s75, s72, 12\n"
"s_waitcnt vmcnt(10)\n"
"s_barrier\n"
"s_cmp_eq_u32 s45, 1\n"
"s_cbranch_scc1 L_res_3_%=\n"
"L_resb_4_%=:\n"
"s_waitcnt lgkmcnt(9)\n"
"v_mfma_f32_32x32x16_bf16 a[100:115], a[116:119], v[80:83], 0\n"
"ds_read_b128 v[160:163], v116 offset:16384\n"
"ds_read_b64_tr_b16 v[96:97], v125 offset:4096\n"
"s_cmp_lg_u32 s95, 0\n"
"s_cselect_b32 s41, s42, 0xfffffd00\n"
"v_add_u32 v112, s41, v112\n"
"v_add_u32 v115, s41, v115\n"
"s_add_u32 m0, s85, 81920\n"
"s_nop 0\n"
"buffer_load_dwordx4 v112, s[4:7], 0 offen offset:0 lds\n"
"v_mfma_f32_32x32x16_bf16 v[16:31], v[144:147], v[80:83], 0\n"
"ds_read_b64_tr_b16 v[98:99], v125 offset:4352\n"
"s_add_u32 s38, s38, s75\n"
"s_lshl_b32 s75, s82, 11\n"
"s_add_u32 s38, s38, s75\n"
"s_add_u32 s38, s38, 2883584\n"
"v_mfma_f32_32x32x16_bf16 v[48:63], v[144:147], v[144:147], 0\n"
"ds_read_b128 v[164:167], v117 offset:16384\n"
"ds_read_b64_tr_b16 v[100:101], v126 offset:4096\n"
"buffer_load_dwordx4 v115, s[4:7], s88 offen offset:1024 lds\n"
"s_waitcnt lgkmcnt(11)\n"
"v_mfma_f32_32x32x16_bf16 v[16:31], v[148:151], v[84:87], v[16:31]\n"
"ds_read_b64_tr_b16 v[102:103], v126 offset:4352\n"
"s_mul_i32 s39, s81, 3584\n"
"s_lshl_b32 s75, s72, 9\n"
"s_add_u32 s39, s39, s75\n"
"s_lshl_b32 s75, s82, 8\n"
"v_mfma_f32_32x32x16_bf16 v[48:63], v[148:151], v[148:151], v[48:63]\n"
"ds_read_b128 v[168:171], v118 offset:16384\n"
"ds_read_b64_tr_b16 v[104:105], v127 offset:4096\n"
"buffer_load_dwordx4 v112, s[4:7], s89 offen offset:2048 lds\n"
"s_waitcnt lgkmcnt(11)\n"
"v_mfma_f32_32x32x16_bf16 v[16:31], v[152:155], v[88:91], v[16:31]\n"
"ds_read_b64_tr_b16 v[106:107], v127 offset:4352\n"
"s_add_u32 s39, s39, s75\n"
"s_add_u32 s39, s39, 4718592\n"
"s_sub_u32 s61, s70, s71\n"
"s_mul_i32 s40, s80, 114688\n"
"v_mfma_f32_32x32x16_bf16 v[48:63], v[152:155], v[152:155], v[48:63]\n"
"ds_read_b128 v[172:175], v119 offset:16384\n"
"ds_read_b64_tr_b16 v[108:109], v128 offset:4096\n"
"buffer_load_dwordx4 v115, s[4:7], s90 offen offset:3072 lds\n"
"s_waitcnt lgkmcnt(11)\n"
"v_mfma_f32_32x32x16_bf16 v[16:31], v[156:159], v[92:95], v[16:31]\n"
"ds_read_b64_tr_b16 v[110:111], v128 offset:4352\n"
"s_lshl_b32 s75, s61, 12\n"
"s_add_u32 s40, s40, s75\n"
"s_add_u32 s40, s40, 6291456\n"
"s_cmp_eq_u32 s72, 7\n"
"s_cselect_b32 s38, 2130706432, s38\n"
"s_cselect_b32 s39, 2130706432, s39\n"
"s_cselect_b32 s40, 2130706432, s40\n"
"v_mfma_f32_32x32x16_bf16 v[48:63], v[156:159], v[156:159], v[48:63]\n"
"s_add_u32 m0, s43, 147456\n"
"s_nop 0\n"
"buffer_load_dwordx4 v113, s[8:11], s95 offen offset:0 lds\n"
"s_mul_i32 s75, s68, 384\n"
"s_add_u32 s75, s75, 57600\n"
"v_lshlrev_b32 v129, 4, v140\n"
"s_waitcnt vmcnt(10)\n"
"s_barrier\n"
"s_cmp_eq_u32 s45, 2\n"
"s_cbranch_scc1 L_res_5_%=\n"
"L_resb_6_%=:\n"
"s_waitcnt lgkmcnt(9)\n"
"v_mfma_f32_32x32x16_bf16 a[100:115], a[116:119], v[96:99], a[100:115]\n"
"ds_read_b128 v[176:179], v116 offset:32768\n"
"v_add_u32 v125, v122, v125\n"
"ds_read_b64_tr_b16 v[80:81], v125 offset:0\n"
"s_cmp_lg_u32 s84, 0\n"
"s_cselect_b32 s41, s42, 0xfffffd00\n"
"v_add_u32 v112, s41, v112\n"
"v_add_u32 v115, s41, v115\n"
"s_add_u32 m0, s85, 98304\n"
"s_nop 0\n"
"buffer_load_dwordx4 v112, s[4:7], 0 offen offset:0 lds\n"
"v_mfma_f32_32x32x16_bf16 v[16:31], v[160:163], v[96:99], v[16:31]\n"
"ds_read_b64_tr_b16 v[82:83], v125 offset:256\n"
"v_add_u32 v129, s75, v129\n"
"v_add_u32 v130, s75, v130\n"
"s_lshl_b32 s75, s68, 2\n"
"v_add_u32 v131, s75, v132\n"
"v_mfma_f32_32x32x16_bf16 v[48:63], v[160:163], v[160:163], v[48:63]\n"
"ds_read_b128 v[180:183], v117 offset:32768\n"
"v_add_u32 v126, v122, v126\n"
"ds_read_b64_tr_b16 v[84:85], v126 offset:0\n"
"buffer_load_dwordx4 v115, s[4:7], s88 offen offset:1024 lds\n"
"s_waitcnt lgkmcnt(11)\n"
"v_mfma_f32_32x32x16_bf16 v[16:31], v[164:167], v[100:103], v[16:31]\n"
"ds_read_b64_tr_b16 v[86:87], v126 offset:256\n"
"v_mov_b32 v13, 0\n"
"v_mov_b32 v14, 0\n"
"v_mov_b32 v15, 0\n"
"s_mov_b32 s54, 0\n"
"v_mfma_f32_32x32x16_bf16 v[48:63], v[164:167], v[164:167], v[48:63]\n"
"ds_read_b128 v[184:187], v118 offset:32768\n"
"v_add_u32 v127, v122, v127\n"
"ds_read_b64_tr_b16 v[88:89], v127 offset:0\n"
"buffer_load_dwordx4 v112, s[4:7], s89 offen offset:2048 lds\n"
"s_waitcnt lgkmcnt(11)\n"
"v_mfma_f32_32x32x16_bf16 v[16:31], v[168:171], v[104:107], v[16:31]\n"
"ds_read_b64_tr_b16 v[90:91], v127 offset:256\n"
"s_mov_b32 s55, -1\n"
"s_mov_b32 s56, 0x10000000\n"
"s_mov_b32 s57, 0x10000000\n"
"s_mov_b32 s58, 0xefffffff\n"
"v_mfma_f32_32x32x16_bf16 v[48:63], v[168:171], v[168:171], v[48:63]\n"
"ds_read_b128 v[188:191], v119 offset:32768\n"
"v_add_u32 v128, v122, v128\n"
"ds_read_b64_tr_b16 v[92:93], v128 offset:0\n"
"buffer_load_dwordx4 v115, s[4:7], s90 offen offset:3072 lds\n"
"s_waitcnt lgkmcnt(11)\n"
"v_mfma_f32_32x32x16_bf16 v[16:31], v[172:175], v[108:111], v[16:31]\n"
"ds_read_b64_tr_b16 v[94:95], v128 offset:256\n"
"s_mov_b32 s59, 0xefffffff\n"
"s_mov_b32 s60, 0x3fb8aa3b\n"
"s_mov_b32 s62, 0x3f317218\n"
"s_mov_b32 s63, 0x3f317218\n"
"v_mfma_f32_32x32x16_bf16 v[48:63], v[172:175], v[172:175], v[48:63]\n"
"s_add_u32 m0, s43, 151552\n"
"s_nop 0\n"
"buffer_load_dwordx4 v113, s[8:11], s84 offen offset:0 lds\n"
"s_mov_b32 s64, 0x358637bd\n"
"s_mov_b32 s46, 0x35c1a2a0\n"
"s_mov_b32 s47, 0x35c1a2a0\n"
"s_lshl_b32 s51, s70, 8\n"
"s_waitcnt vmcnt(10)\n"
"s_barrier\n"
"s_cmp_eq_u32 s45, 3\n"
"s_cbranch_scc1 L_res_7_%=\n"
"L_resb_8_%=:\n"
"s_waitcnt lgkmcnt(9)\n"
"v_mfma_f32_32x32x16_bf16 a[100:115], a[116:119], v[80:83], a[100:115]\n"
"v_add_u32 v116, 65536, v116\n"
"ds_read_b128 v[192:195], v116 offset:0\n"
"ds_read_b64_tr_b16 v[96:97], v125 offset:4096\n"
"s_cmp_lg_u32 s86, 0\n"
"s_cselect_b32 s41, s42, 0xfffffd00\n"
"v_add_u32 v112, s41, v112\n"
"v_add_u32 v115, s41, v115\n"
"s_add_u32 m0, s85, 114688\n"
"s_nop 0\n"
"buffer_load_dwordx4 v112, s[4:7], 0 offen offset:0 lds\n"
"v_mfma_f32_32x32x16_bf16 v[16:31], v[176:179], v[80:83], v[16:31]\n"
"ds_read_b64_tr_b16 v[98:99], v125 offset:4352\n"
"s_lshl_b32 s75, s69, 4\n"
"s_add_u32 s51, s51, s75\n"
"s_lshl_b32 s75, s68, 2\n"
"s_add_u32 s51, s51, s75\n"
"v_mfma_f32_32x32x16_bf16 v[48:63], v[176:179], v[176:179], v[48:63]\n"
"v_add_u32 v117, 65536, v117\n"
"ds_read_b128 v[196:199], v117 offset:0\n"
"ds_read_b64_tr_b16 v[100:101], v126 offset:4096\n"
"buffer_load_dwordx4 v115, s[4:7], s88 offen offset:1024 lds\n"
"s_waitcnt lgkmcnt(11)\n"
"v_mfma_f32_32x32x16_bf16 v[16:31], v[180:183], v[84:87], v[16:31]\n"
"ds_read_b64_tr_b16 v[102:103], v126 offset:4352\n"
"s_add_u32 s51, s51, 4096\n"
"v_mov_b32 v139, 0x9E3779B1\n"
"s_mov_b32 s14, 401408\n"
"s_mov_b32 s15, 0x20000\n"
"v_mfma_f32_32x32x16_bf16 v[48:63], v[180:183], v[180:183], v[48:63]\n"
"v_add_u32 v118, 65536, v118\n"
"ds_read_b128 v[200:203], v118 offset:0\n"
"ds_read_b64_tr_b16 v[104:105], v127 offset:4096\n"
"buffer_load_dwordx4 v112, s[4:7], s89 offen offset:2048 lds\n"
"s_waitcnt lgkmcnt(11)\n"
"v_mfma_f32_32x32x16_bf16 v[16:31], v[184:187], v[88:91], v[16:31]\n"
"ds_read_b64_tr_b16 v[106:107], v127 offset:4352\n"
"s_mov_b32 s22, 1605632\n"
"s_mov_b32 s23, 0x20000\n"
"s_mov_b32 s30, 1835008\n"
"s_mov_b32 s31, 0x20000\n"
"v_mfma_f32_32x32x16_bf16 v[48:63], v[184:187], v[184:187], v[48:63]\n"
"v_add_u32 v119, 65536, v119\n"
"ds_read_b128 v[204:207], v119 offset:0\n"
"ds_read_b64_tr_b16 v[108:109], v128 offset:4096\n"
"buffer_load_dwordx4 v115, s[4:7], s90 offen offset:3072 lds\n"
"s_waitcnt lgkmcnt(11)\n"
"v_mfma_f32_32x32x16_bf16 v[16:31], v[188:191], v[92:95], v[16:31]\n"
"ds_read_b64_tr_b16 v[110:111], v128 offset:4352\n"
"s_mov_b32 s18, 1605632\n"
"s_mov_b32 s19, 0x20000\n"
"v_mov_b32 v68, 0\n"
"v_mfma_f32_32x32x16_bf16 v[48:63], v[188:191], v[188:191], v[48:63]\n"
"s_add_u32 m0, s43, 155648\n"
"s_nop 0\n"
"buffer_load_dwordx4 v113, s[8:11], s86 offen offset:0 lds\n"
"v_lshlrev_b32 v3, 2, v1\n"
"ds_write_b32 v3, v68 offset:57344\n"
"v_bfe_u32 v2, v1, 2, 1\n"
"v_bfe_u32 v3, v1, 3, 1\n"
"s_waitcnt vmcnt(10)\n"
"s_barrier\n"
"s_cmp_eq_u32 s45, 4\n"
"s_cbranch_scc1 L_res_9_%=\n"
"L_resb_10_%=:\n"
"s_waitcnt lgkmcnt(10)\n"
"v_mfma_f32_32x32x16_bf16 a[100:115], a[116:119], v[96:99], a[100:115]\n"
"ds_read_b128 v[208:211], v116 offset:16384\n"
"v_add_u32 v125, v122, v125\n"
"ds_read_b64_tr_b16 v[80:81], v125 offset:0\n"
"v_add_u32 v113, 896, v113\n"
"s_mov_b32 m0, s43\n"
"s_nop 0\n"
"buffer_load_dwordx4 v113, s[8:11], s78 offen offset:0 lds\n"
"v_mfma_f32_32x32x16_bf16 v[16:31], v[192:195], v[96:99], v[16:31]\n"
"ds_read_b64_tr_b16 v[82:83], v125 offset:256\n"
"v_lshl_add_u32 v32, v2, 1, v3\n"
"v_and_b32 v141, 3, v1\n"
"v_lshl_add_u32 v32, v32, 2, v141\n"
"v_lshl_add_u32 v3, v3, 2, v141\n"
"v_mfma_f32_32x32x16_bf16 v[48:63], v[192:195], v[192:195], v[48:63]\n"
"ds_read_b128 v[212:215], v117 offset:16384\n"
"v_add_u32 v126, v122, v126\n"
"ds_read_b64_tr_b16 v[84:85], v126 offset:0\n"
"s_add_u32 m0, s43, 4096\n"
"s_nop 0\n"
"buffer_load_dwordx4 v113, s[8:11], s50 offen offset:0 lds\n"
"s_waitcnt lgkmcnt(12)\n"
"v_mfma_f32_32x32x16_bf16 v[16:31], v[196:199], v[100:103], v[16:31]\n"
"ds_read_b64_tr_b16 v[86:87], v126 offset:256\n"
"v_cmp_eq_u32 vcc, 7, v3\n"
"v_mad_u32_u24 v33, v2, 7, v3\n"
"s_mul_i32 s75, s82, 14\n"
"v_add_u32 v33, s75, v33\n"
"v_mfma_f32_32x32x16_bf16 v[48:63], v[196:199], v[196:199], v[48:63]\n"
"ds_read_b128 v[216:219], v118 offset:16384\n"
"v_add_u32 v127, v122, v127\n"
"ds_read_b64_tr_b16 v[88:89], v127 offset:0\n"
"buffer_load_dword v137, off, s[24:27], 0 sc1\n"
"s_waitcnt lgkmcnt(12)\n"
"v_mfma_f32_32x32x16_bf16 v[16:31], v[200:203], v[104:107], v[16:31]\n"
"ds_read_b64_tr_b16 v[90:91], v127 offset:256\n"
"v_cndmask_b32 v33, v33, 28, vcc\n"
"v_bfe_u32 v2, v1, 4, 1\n"
"v_lshlrev_b32 v3, 9, v2\n"
"v_lshl_add_u32 v3, v140, 10, v3\n"
"v_mfma_f32_32x32x16_bf16 v[48:63], v[200:203], v[200:203], v[48:63]\n"
"ds_read_b128 v[220:223], v119 offset:16384\n"
"v_add_u32 v128, v122, v128\n"
"ds_read_b64_tr_b16 v[92:93], v128 offset:0\n"
"s_mov_b64 exec, s[52:53]\n"
"buffer_store_dwordx4 a[204:207], v132, s[24:27], s36 offen sc1\n"
"s_mov_b64 exec, -1\n"
"s_waitcnt lgkmcnt(12)\n"
"v_mfma_f32_32x32x16_bf16 v[16:31], v[204:207], v[108:111], v[16:31]\n"
"ds_read_b64_tr_b16 v[94:95], v128 offset:256\n"
"v_add_u32 v3, 147456, v3\n"
"v_lshl_add_u32 v33, v33, 4, v3\n"
"v_accvgpr_write_b32 a125, v33\n"
"s_lshl_b32 s75, s83, 8\n"
"v_mfma_f32_32x32x16_bf16 v[48:63], v[204:207], v[204:207], v[48:63]\n"
"v_lshl_add_u32 v32, v32, 4, v3\n"
"v_add_u32 v32, s75, v32\n"
"v_accvgpr_write_b32 a124, v32\n"
"v_lshrrev_b32 v32, 2, v1\n"
"s_waitcnt vmcnt(9)\n"
"s_barrier\n"
"s_cmp_eq_u32 s45, 5\n"
"s_cbranch_scc1 L_res_11_%=\n"
"L_resb_12_%=:\n"
"s_waitcnt lgkmcnt(9)\n"
"v_mfma_f32_32x32x16_bf16 a[100:115], a[116:119], v[80:83], a[100:115]\n"
"ds_read_b128 v[224:227], v116 offset:32768\n"
"ds_read_b64_tr_b16 v[96:97], v125 offset:4096\n"
"s_add_u32 m0, s43, 8192\n"
"s_nop 0\n"
"buffer_load_dwordx4 v113, s[8:11], s35 offen offset:0 lds\n"
"v_mfma_f32_32x32x16_bf16 v[16:31], v[208:211], v[80:83], v[16:31]\n"
"ds_read_b64_tr_b16 v[98:99], v125 offset:4352\n"
"v_xor_b32 v32, v32, v2\n"
"v_and_b32 v32, 1, v32\n"
"v_and_b32 v33, 48, v132\n"
"v_lshl_add_u32 v32, v32, 6, v33\n"
"v_mfma_f32_32x32x16_bf16 v[48:63], v[208:211], v[208:211], v[48:63]\n"
"ds_read_b128 v[228:231], v117 offset:32768\n"
"ds_read_b64_tr_b16 v[100:101], v126 offset:4096\n"
"buffer_store_dwordx4 a[204:207], v132, s[24:27], s37 offen offset:0 sc1\n"
"s_waitcnt lgkmcnt(11)\n"
"v_mfma_f32_32x32x16_bf16 v[16:31], v[212:215], v[84:87], v[16:31]\n"
"ds_read_b64_tr_b16 v[102:103], v126 offset:4352\n"
"v_lshrrev_b32 v33, 3, v1\n"
"v_mul_u32_u24 v3, 896, v33\n"
"v_add_u32 v32, v32, v3\n"
"v_accvgpr_write_b32 a127, v32\n"
"v_mfma_f32_32x32x16_bf16 v[48:63], v[212:215], v[212:215], v[48:63]\n"
"ds_read_b128 v[232:235], v118 offset:32768\n"
"ds_read_b64_tr_b16 v[104:105], v127 offset:4096\n"
"v_lshlrev_b32 v143, 7, v1\n"
"s_bfm_b32 s44, s70, 0\n"
"s_and_b32 s44, s44, 10836\n"
"s_bcnt1_i32_b32 s44, s44\n"
"s_sub_u32 s44, s70, s44\n"
"s_mul_i32 s42, s69, 25600\n"
"s_mul_i32 s41, s44, 2560\n"
"s_add_u32 s42, s42, s41\n"
"s_mul_i32 s41, s68, 640\n"
"s_add_u32 s42, s42, s41\n"
"s_lshr_b32 s44, 0x2a54, s70\n"
"s_bitcmp1_b32 s44, 0\n"
"s_cselect_b32 s42, 2130706432, s42\n"
"s_mov_b64 exec, 0x1f\n"
"buffer_load_dword a196, v143, s[12:15], s42 offen\n"
"s_mov_b64 exec, -1\n"
"s_waitcnt lgkmcnt(11)\n"
"v_mfma_f32_32x32x16_bf16 v[16:31], v[216:219], v[88:91], v[16:31]\n"
"ds_read_b64_tr_b16 v[106:107], v127 offset:4352\n"
"s_mul_i32 s48, s68, 100352\n"
"s_lshl_b32 s75, s72, 7\n"
"s_add_u32 s48, s48, s75\n"
"s_mul_i32 s49, s68, 14336\n"
"v_mfma_f32_32x32x16_bf16 v[48:63], v[216:219], v[216:219], v[48:63]\n"
"ds_read_b128 v[236:239], v119 offset:32768\n"
"ds_read_b64_tr_b16 v[108:109], v128 offset:4096\n"
"s_waitcnt lgkmcnt(11)\n"
"v_mfma_f32_32x32x16_bf16 v[16:31], v[220:223], v[92:95], v[16:31]\n"
"ds_read_b64_tr_b16 v[110:111], v128 offset:4352\n"
"v_mfma_f32_32x32x16_bf16 v[48:63], v[220:223], v[220:223], v[48:63]\n"
"s_waitcnt vmcnt(7)\n"
"s_barrier\n"
"s_cmp_eq_u32 s45, 6\n"
"s_cbranch_scc1 L_res_13_%=\n"
"L_resb_14_%=:\n"
"s_waitcnt lgkmcnt(9)\n"
"v_mfma_f32_32x32x16_bf16 a[100:115], a[116:119], v[96:99], a[100:115]\n"
"ds_read_b128 v[240:243], v116 offset:49152\n"
"v_add_u32 v125, v122, v125\n"
"ds_read_b64_tr_b16 v[80:81], v125 offset:0\n"
"s_add_u32 m0, s43, 12288\n"
"s_nop 0\n"
"buffer_load_dwordx4 v113, s[8:11], s79 offen offset:0 lds\n"
"v_mfma_f32_32x32x16_bf16 v[16:31], v[224:227], v[96:99], v[16:31]\n"
"ds_read_b64_tr_b16 v[82:83], v125 offset:256\n"
"v_mfma_f32_32x32x16_bf16 v[48:63], v[224:227], v[224:227], v[48:63]\n"
"ds_read_b128 v[244:247], v117 offset:49152\n"
"v_add_u32 v126, v122, v126\n"
"ds_read_b64_tr_b16 v[84:85], v126 offset:0\n"
"buffer_store_dwordx4 a[204:207], v132, s[24:27], s37 offen offset:1024 sc1\n"
"s_waitcnt lgkmcnt(11)\n"
"v_mfma_f32_32x32x16_bf16 v[16:31], v[228:231], v[100:103], v[16:31]\n"
"ds_read_b64_tr_b16 v[86:87], v126 offset:256\n"
"v_mfma_f32_32x32x16_bf16 v[48:63], v[228:231], v[228:231], v[48:63]\n"
"v_accvgpr_read_b32 v133, a100\n"
"ds_write_b32 v131, v133 offset:59136\n"
"ds_read_b128 v[248:251], v118 offset:49152\n"
"v_add_u32 v127, v122, v127\n"
"ds_read_b64_tr_b16 v[88:89], v127 offset:0\n"
"s_waitcnt lgkmcnt(12)\n"
"v_mfma_f32_32x32x16_bf16 v[16:31], v[232:235], v[104:107], v[16:31]\n"
"ds_read_b64_tr_b16 v[90:91], v127 offset:256\n"
"v_mfma_f32_32x32x16_bf16 v[48:63], v[232:235], v[232:235], v[48:63]\n"
"ds_read_b128 v[252:255], v119 offset:49152\n"
"v_add_u32 v128, v122, v128\n"
"ds_read_b64_tr_b16 v[92:93], v128 offset:0\n"
"s_waitcnt lgkmcnt(12)\n"
"v_mfma_f32_32x32x16_bf16 v[16:31], v[236:239], v[108:111], v[16:31]\n"
"ds_read_b64_tr_b16 v[94:95], v128 offset:256\n"
"v_mfma_f32_32x32x16_bf16 v[48:63], v[236:239], v[236:239], v[48:63]\n"
"s_waitcnt vmcnt(8)\n"
"s_waitcnt lgkmcnt(0)\n"
"s_barrier\n"
"v_mfma_f32_32x32x16_bf16 a[100:115], a[116:119], v[80:83], 0\n"
"ds_read_b128 v[76:79], v132 offset:59136\n"
"v_add_u32 v134, 0, v123\n"
"v_add_u32 v125, s91, v134\n"
"v_add_u32 v126, s92, v134\n"
"v_add_u32 v127, s93, v134\n"
"v_add_u32 v128, s94, v134\n"
"v_mfma_f32_32x32x16_bf16 a[100:115], a[116:119], v[84:87], a[100:115]\n"
"ds_read_b64_tr_b16 v[96:97], v125 offset:0\n"
"ds_read_b64_tr_b16 v[98:99], v125 offset:256\n"
"ds_read_b64_tr_b16 v[100:101], v126 offset:0\n"
"ds_read_b64_tr_b16 v[102:103], v126 offset:256\n"
"s_add_u32 m0, s43, 16384\n"
"s_nop 0\n"
"buffer_load_dwordx4 v113, s[8:11], s95 offen offset:0 lds\n"
"v_mfma_f32_32x32x16_bf16 a[100:115], a[116:119], v[88:91], a[100:115]\n"
"ds_read_b64_tr_b16 v[104:105], v127 offset:0\n"
"ds_read_b64_tr_b16 v[106:107], v127 offset:256\n"
"ds_read_b64_tr_b16 v[108:109], v128 offset:0\n"
"ds_read_b64_tr_b16 v[110:111], v128 offset:256\n"
"buffer_store_dwordx4 a[204:207], v132, s[24:27], s38 offen offset:0 sc1\n"
"v_mfma_f32_32x32x16_bf16 a[100:115], a[116:119], v[92:95], a[100:115]\n"
"v_mfma_f32_32x32x16_bf16 v[16:31], v[240:243], v[80:83], v[16:31]\n"
"s_waitcnt lgkmcnt(8)\n"
"v_add_f32 v135, v76, v77\n"
"v_add_f32 v133, v78, v79\n"
"v_add_f32 v135, v135, v133\n"
"v_mfma_f32_32x32x16_bf16 v[16:31], v[244:247], v[84:87], v[16:31]\n"
"v_mfma_f32_32x32x16_bf16 v[16:31], v[248:251], v[88:91], v[16:31]\n"
"s_nop 4\n"
"v_accvgpr_read_b32 v134, a100\n"
"v_add_f32 v135, v135, v134\n"
"v_mfma_f32_32x32x16_bf16 v[16:31], v[252:255], v[92:95], v[16:31]\n"
"v_cvt_pk_bf16_f32 v133, v135, v135\n"
"v_lshlrev_b32 v133, 16, v133\n"
"v_sub_f32 v12, v135, v133\n"
"v_cvt_pk_bf16_f32 v12, v133, v12\n"
"v_mfma_f32_32x32x16_bf16 v[48:63], v[240:243], v[240:243], v[48:63]\n"
"v_mfma_f32_32x32x16_bf16 v[48:63], v[244:247], v[244:247], v[48:63]\n"
"s_mov_b64 exec, s[56:57]\n"
"s_nop 4\n"
"ds_write_b128 v129, v[16:19] offset:0\n"
"s_mov_b64 exec, -1\n"
"v_mfma_f32_32x32x16_bf16 v[48:63], v[248:251], v[248:251], v[48:63]\n"
"s_mov_b64 exec, s[56:57]\n"
"ds_write_b128 v129, v[20:23] offset:32\n"
"s_mov_b64 exec, -1\n"
"v_mfma_f32_32x32x16_bf16 v[48:63], v[252:255], v[252:255], v[48:63]\n"
"s_mov_b64 exec, s[56:57]\n"
"ds_write_b128 v129, v[24:27] offset:64\n"
"ds_write_b128 v129, v[28:31] offset:96\n"
"s_mov_b64 exec, -1\n"
"ds_read_b32 v72, v130\n"
"s_waitcnt vmcnt(9)\n"
"s_barrier\n"
"s_waitcnt lgkmcnt(11)\n"
"v_mfma_f32_32x32x16_bf16 a[100:115], a[116:119], v[96:99], 0\n"
"ds_read_b64_tr_b16 v[80:81], v125 offset:4096\n"
"ds_read_b64_tr_b16 v[82:83], v125 offset:4352\n"
"v_mov_b32 v117, 0\n"
"v_mov_b32 v118, 0\n"
"v_mov_b32 v119, 0\n"
"s_waitcnt lgkmcnt(2)\n"
"v_mul_f32 v74, 0xbb124925, v72\n"
"v_cvt_pk_bf16_f32 v2, v74, v74\n"
"v_mfma_f32_32x32x16_bf16 v[32:47], v[144:147], v[96:99], 0\n"
"ds_read_b64_tr_b16 v[84:85], v126 offset:4096\n"
"ds_read_b64_tr_b16 v[86:87], v126 offset:4352\n"
"s_add_u32 m0, s43, 20480\n"
"s_nop 0\n"
"buffer_load_dwordx4 v113, s[8:11], s84 offen offset:0 lds\n"
"v_lshlrev_b32 v3, 16, v2\n"
"v_sub_f32 v3, v74, v3\n"
"v_cvt_pk_bf16_f32 v3, v3, 0\n"
"v_cndmask_b32 v116, v2, v3, s[54:55]\n"
"s_mov_b32 vcc_lo, 0xaaaaaaaa\n"
"s_mov_b32 vcc_hi, 0xaaaaaaaa\n"
"v_mfma_f32_32x32x16_bf16 v[32:47], v[148:151], v[100:103], v[32:47]\n"
"ds_read_b64_tr_b16 v[88:89], v127 offset:4096\n"
"ds_read_b64_tr_b16 v[90:91], v127 offset:4352\n"
"v_cndmask_b32 v48, v48, v49, vcc\n"
"v_cndmask_b32 v50, v50, v51, vcc\n"
"v_mfma_f32_32x32x16_bf16 v[16:31], v[116:119], v[12:15], v[16:31]\n"
"v_cndmask_b32 v52, v52, v53, vcc\n"
"v_cndmask_b32 v54, v54, v55, vcc\n"
"v_mfma_f32_32x32x16_bf16 v[32:47], v[152:155], v[104:107], v[32:47]\n"
"ds_read_b64_tr_b16 v[92:93], v128 offset:4096\n"
"ds_read_b64_tr_b16 v[94:95], v128 offset:4352\n"
"buffer_store_dwordx4 a[204:207], v132, s[24:27], s38 offen offset:1024 sc1\n"
"v_cndmask_b32 v56, v56, v57, vcc\n"
"v_cndmask_b32 v58, v58, v59, vcc\n"
"v_cndmask_b32 v60, v60, v61, vcc\n"
"v_cndmask_b32 v62, v62, v63, vcc\n"
"s_mov_b32 vcc_lo, 0xcccccccc\n"
"s_mov_b32 vcc_hi, 0xcccccccc\n"
"v_mfma_f32_32x32x16_bf16 v[32:47], v[156:159], v[108:111], v[32:47]\n"
"v_cndmask_b32 v48, v48, v50, vcc\n"
"v_cndmask_b32 v52, v52, v54, vcc\n"
"v_cndmask_b32 v56, v56, v58, vcc\n"
"v_cndmask_b32 v60, v60, v62, vcc\n"
"s_mov_b32 vcc_lo, 0xff00ff00\n"
"s_mov_b32 vcc_hi, 0xff00ff00\n"
"s_waitcnt vmcnt(9)\n"
"v_mul_lo_u32 v137, v137, v139\n"
"v_add_u32 v137, 0x7F4A7C15, v137\n"
"s_mov_b64 exec, 1\n"
"buffer_store_dword v137, off, s[24:27], s51 sc1\n"
"s_mov_b64 exec, -1\n"
"s_waitcnt vmcnt(9)\n"
"s_barrier\n"
"s_waitcnt lgkmcnt(6)\n"
"v_mfma_f32_32x32x16_bf16 a[100:115], a[116:119], v[80:83], a[100:115]\n"
"ds_read_b64_tr_b16 v[96:97], v125 offset:8192\n"
"ds_read_b64_tr_b16 v[98:99], v125 offset:8448\n"
"v_cndmask_b32 v48, v48, v52, vcc\n"
"v_cndmask_b32 v56, v56, v60, vcc\n"
"s_mov_b32 vcc_lo, 0xffff0000\n"
"s_mov_b32 vcc_hi, 0xffff0000\n"
"v_cndmask_b32 v48, v48, v56, vcc\n"
"v_mov_b32 v49, v48\n"
"v_mfma_f32_32x32x16_bf16 v[32:47], v[160:163], v[80:83], v[32:47]\n"
"ds_read_b64_tr_b16 v[100:101], v126 offset:8192\n"
"ds_read_b64_tr_b16 v[102:103], v126 offset:8448\n"
"s_add_u32 m0, s43, 24576\n"
"s_nop 0\n"
"buffer_load_dwordx4 v113, s[8:11], s86 offen offset:0 lds\n"
"s_mov_b32 vcc_lo, 0xf0f0f0f0\n"
"s_mov_b32 vcc_hi, 0xf0f0f0f0\n"
"v_permlane32_swap_b32 v49, v48\n"
"v_cndmask_b32 v73, v49, v48, vcc\n"
"v_mul_f32 v73, 0x3b124925, v73\n"
"v_fma_f32 v73, -v74, v74, v73\n"
"s_waitcnt lgkmcnt(8)\n"
"v_mfma_f32_32x32x16_bf16 v[32:47], v[164:167], v[84:87], v[32:47]\n"
"ds_read_b64_tr_b16 v[104:105], v127 offset:8192\n"
"ds_read_b64_tr_b16 v[106:107], v127 offset:8448\n"
"v_max_f32 v73, 0, v73\n"
"v_add_f32 v73, 0x3727c5ac, v73\n"
"v_rsq_f32 v73, v73\n"
"v_accvgpr_read_b32 v112, a126\n"
"v_mul_f32 v136, s60, v73\n"
"s_waitcnt lgkmcnt(8)\n"
"v_mfma_f32_32x32x16_bf16 v[32:47], v[168:171], v[88:91], v[32:47]\n"
"ds_read_b64_tr_b16 v[108:109], v128 offset:8192\n"
"ds_read_b64_tr_b16 v[110:111], v128 offset:8448\n"
"v_lshlrev_b32 v143, 3, v1\n"
"s_mov_b64 exec, s[52:53]\n"
"buffer_store_dwordx2 a[204:205], v143, s[24:27], s39 offen sc1\n"
"s_mov_b64 exec, -1\n"
"ds_write_b32 v130, v136 offset:256\n"
"ds_write_b32 v130, v73 offset:128\n"
"ds_read_b128 v[48:51], v129 offset:256\n"
"ds_read_b128 v[52:55], v129 offset:288\n"
"ds_read_b128 v[56:59], v129 offset:320\n"
"s_waitcnt lgkmcnt(13)\n"
"v_mfma_f32_32x32x16_bf16 v[32:47], v[172:175], v[92:95], v[32:47]\n"
"ds_read_b128 v[60:63], v129 offset:352\n"
"v_accvgpr_read_b32 v114, a124\n"
"v_accvgpr_read_b32 v115, a125\n"
"v_mov_b32 v72, s77\n"
"v_mov_b32 v73, s77\n"
"s_waitcnt vmcnt(8)\n"
"s_barrier\n"
"s_waitcnt lgkmcnt(12)\n"
"v_mfma_f32_32x32x16_bf16 a[100:115], a[116:119], v[96:99], a[100:115]\n"
"ds_read_b64_tr_b16 v[80:81], v125 offset:12288\n"
"ds_read_b64_tr_b16 v[82:83], v125 offset:12544\n"
"v_mov_b32 v74, s77\n"
"v_mov_b32 v75, s77\n"
"s_waitcnt lgkmcnt(5)\n"
"v_fma_f32 v16, v16, v48, s46\n"
"v_fma_f32 v17, v17, v49, s46\n"
"v_exp_f32_e64 v2, v16 clamp\n"
"v_mfma_f32_32x32x16_bf16 v[32:47], v[176:179], v[96:99], v[32:47]\n"
"ds_read_b64_tr_b16 v[84:85], v126 offset:12288\n"
"ds_read_b64_tr_b16 v[86:87], v126 offset:12544\n"
"v_subrev_u32 v113, 1792, v113\n"
"s_add_u32 m0, s43, 65536\n"
"s_nop 0\n"
"buffer_load_dwordx4 v113, s[8:11], s78 offen offset:0 lds\n"
"v_max_f32 v16, s46, v16\n"
"v_exp_f32_e64 v3, v17 clamp\n"
"v_max_f32 v17, s46, v17\n"
"v_fmac_f32 v2, s62, v16\n"
"v_fmac_f32 v3, s62, v17\n"
"v_mfma_f32_32x32x16_bf16 v[32:47], v[180:183], v[100:103], v[32:47]\n"
"ds_read_b64_tr_b16 v[88:89], v127 offset:12288\n"
"ds_read_b64_tr_b16 v[90:91], v127 offset:12544\n"
"v_cvt_pk_bf16_f32 v64, v2, v3\n"
"v_fma_f32 v18, v18, v50, s46\n"
"v_fma_f32 v19, v19, v51, s46\n"
"v_exp_f32_e64 v2, v18 clamp\n"
"v_max_f32 v18, s46, v18\n"
"v_mfma_f32_32x32x16_bf16 v[32:47], v[184:187], v[104:107], v[32:47]\n"
"ds_read_b64_tr_b16 v[92:93], v128 offset:12288\n"
"ds_read_b64_tr_b16 v[94:95], v128 offset:12544\n"
"v_exp_f32_e64 v3, v19 clamp\n"
"v_max_f32 v19, s46, v19\n"
"v_fmac_f32 v2, s62, v18\n"
"v_fmac_f32 v3, s62, v19\n"
"v_cvt_pk_bf16_f32 v65, v2, v3\n"
"v_mfma_f32_32x32x16_bf16 v[32:47], v[188:191], v[108:111], v[32:47]\n"
"s_waitcnt lgkmcnt(10)\n"
"v_fma_f32 v20, v20, v52, s46\n"
"v_fma_f32 v21, v21, v53, s46\n"
"v_exp_f32_e64 v2, v20 clamp\n"
"v_max_f32 v20, s46, v20\n"
"v_exp_f32_e64 v3, v21 clamp\n"
"s_waitcnt vmcnt(7)\n"
"s_barrier\n"
"s_waitcnt lgkmcnt(6)\n"
"v_mfma_f32_32x32x16_bf16 a[100:115], a[116:119], v[80:83], a[100:115]\n"
"ds_read_b64_tr_b16 v[96:97], v125 offset:16384\n"
"ds_read_b64_tr_b16 v[98:99], v125 offset:16640\n"
"v_max_f32 v21, s46, v21\n"
"v_fmac_f32 v2, s62, v20\n"
"v_fmac_f32 v3, s62, v21\n"
"v_cvt_pk_bf16_f32 v66, v2, v3\n"
"v_fma_f32 v22, v22, v54, s46\n"
"v_mfma_f32_32x32x16_bf16 v[32:47], v[192:195], v[80:83], v[32:47]\n"
"ds_read_b64_tr_b16 v[100:101], v126 offset:16384\n"
"ds_read_b64_tr_b16 v[102:103], v126 offset:16640\n"
"s_add_u32 m0, s43, 69632\n"
"s_nop 0\n"
"buffer_load_dwordx4 v113, s[8:11], s50 offen offset:0 lds\n"
"v_fma_f32 v23, v23, v55, s46\n"
"v_exp_f32_e64 v2, v22 clamp\n"
"v_max_f32 v22, s46, v22\n"
"v_exp_f32_e64 v3, v23 clamp\n"
"v_max_f32 v23, s46, v23\n"
"s_waitcnt lgkmcnt(8)\n"
"v_mfma_f32_32x32x16_bf16 v[32:47], v[196:199], v[84:87], v[32:47]\n"
"ds_read_b64_tr_b16 v[104:105], v127 offset:16384\n"
"ds_read_b64_tr_b16 v[106:107], v127 offset:16640\n"
"v_fmac_f32 v2, s62, v22\n"
"v_fmac_f32 v3, s62, v23\n"
"v_cvt_pk_bf16_f32 v67, v2, v3\n"
"ds_write_b128 v112, v[64:67] offset:0\n"
"ds_read_b128 v[48:51], v129 offset:128\n"
"s_waitcnt lgkmcnt(10)\n"
"v_mfma_f32_32x32x16_bf16 v[32:47], v[200:203], v[88:91], v[32:47]\n"
"ds_read_b64_tr_b16 v[108:109], v128 offset:16384\n"
"ds_read_b64_tr_b16 v[110:111], v128 offset:16640\n"
"s_add_u32 m0, s43, 73728\n"
"s_nop 0\n"
"buffer_load_dwordx4 v113, s[8:11], s35 offen offset:0 lds\n"
"ds_read_b128 v[52:55], v129 offset:160\n"
"v_fma_f32 v24, v24, v56, s46\n"
"v_fma_f32 v25, v25, v57, s46\n"
"v_exp_f32_e64 v2, v24 clamp\n"
"v_max_f32 v24, s46, v24\n"
"s_waitcnt lgkmcnt(11)\n"
"v_mfma_f32_32x32x16_bf16 v[32:47], v[204:207], v[92:95], v[32:47]\n"
"v_exp_f32_e64 v3, v25 clamp\n"
"v_max_f32 v25, s46, v25\n"
"v_fmac_f32 v2, s62, v24\n"
"v_fmac_f32 v3, s62, v25\n"
"v_cvt_pk_bf16_f32 v68, v2, v3\n"
"s_waitcnt vmcnt(7)\n"
"s_barrier\n"
"s_waitcnt lgkmcnt(9)\n"
"v_mfma_f32_32x32x16_bf16 a[100:115], a[116:119], v[96:99], a[100:115]\n"
"ds_read_b64_tr_b16 v[80:81], v125 offset:20480\n"
"ds_read_b64_tr_b16 v[82:83], v125 offset:20736\n"
"v_fma_f32 v26, v26, v58, s46\n"
"v_fma_f32 v27, v27, v59, s46\n"
"v_exp_f32_e64 v2, v26 clamp\n"
"v_max_f32 v26, s46, v26\n"
"v_exp_f32_e64 v3, v27 clamp\n"
"v_mfma_f32_32x32x16_bf16 v[32:47], v[208:211], v[96:99], v[32:47]\n"
"ds_read_b64_tr_b16 v[84:85], v126 offset:20480\n"
"ds_read_b64_tr_b16 v[86:87], v126 offset:20736\n"
"s_add_u32 m0, s43, 77824\n"
"s_nop 0\n"
"buffer_load_dwordx4 v113, s[8:11], s79 offen offset:0 lds\n"
"v_max_f32 v27, s46, v27\n"
"v_fmac_f32 v2, s62, v26\n"
"v_fmac_f32 v3, s62, v27\n"
"v_cvt_pk_bf16_f32 v69, v2, v3\n"
"v_fma_f32 v28, v28, v60, s46\n"
"s_waitcnt lgkmcnt(11)\n"
"v_mfma_f32_32x32x16_bf16 v[32:47], v[212:215], v[100:103], v[32:47]\n"
"ds_read_b64_tr_b16 v[88:89], v127 offset:20480\n"
"ds_read_b64_tr_b16 v[90:91], v127 offset:20736\n"
"v_fma_f32 v29, v29, v61, s46\n"
"v_exp_f32_e64 v2, v28 clamp\n"
"v_max_f32 v28, s46, v28\n"
"v_exp_f32_e64 v3, v29 clamp\n"
"v_max_f32 v29, s46, v29\n"
"s_waitcnt lgkmcnt(11)\n"
"v_mfma_f32_32x32x16_bf16 v[32:47], v[216:219], v[104:107], v[32:47]\n"
"ds_read_b64_tr_b16 v[92:93], v128 offset:20480\n"
"ds_read_b64_tr_b16 v[94:95], v128 offset:20736\n"
"s_add_u32 m0, s43, 81920\n"
"s_nop 0\n"
"buffer_load_dwordx4 v113, s[8:11], s95 offen offset:0 lds\n"
"v_fmac_f32 v2, s62, v28\n"
"v_fmac_f32 v3, s62, v29\n"
"v_cvt_pk_bf16_f32 v70, v2, v3\n"
"v_fma_f32 v30, v30, v62, s46\n"
"v_fma_f32 v31, v31, v63, s46\n"
"s_waitcnt lgkmcnt(9)\n"
"v_mfma_f32_32x32x16_bf16 v[32:47], v[220:223], v[108:111], v[32:47]\n"
"v_exp_f32_e64 v2, v30 clamp\n"
"v_max_f32 v30, s46, v30\n"
"v_exp_f32_e64 v3, v31 clamp\n"
"v_max_f32 v31, s46, v31\n"
"v_fmac_f32 v2, s62, v30\n"
"s_lshl_b32 s51, s70, 13\n"
"s_lshl_b32 s42, s69, 9\n"
"s_add_u32 s51, s51, s42\n"
"s_lshl_b32 s42, s68, 7\n"
"s_add_u32 s51, s51, s42\n"
"s_add_u32 s51, s51, 4964352\n"
"s_waitcnt vmcnt(5)\n"
"v_lshlrev_b32 v143, 2, v1\n"
"s_mov_b64 exec, s[52:53]\n"
"buffer_store_dword v137, v143, s[24:27], s51 offen sc1\n"
"s_mov_b64 exec, -1\n"
"s_barrier\n"
"s_waitcnt lgkmcnt(6)\n"
"v_mfma_f32_32x32x16_bf16 a[100:115], a[116:119], v[80:83], a[100:115]\n"
"ds_read_b64_tr_b16 v[96:97], v125 offset:24576\n"
"ds_read_b64_tr_b16 v[98:99], v125 offset:24832\n"
"v_fmac_f32 v3, s62, v31\n"
"v_cvt_pk_bf16_f32 v71, v2, v3\n"
"ds_write_b128 v112, v[68:71] offset:1024\n"
"ds_read_b128 v[56:59], v129 offset:192\n"
"ds_read_b128 v[60:63], v129 offset:224\n"
"v_mfma_f32_32x32x16_bf16 v[32:47], v[224:227], v[80:83], v[32:47]\n"
"ds_read_b64_tr_b16 v[100:101], v126 offset:24576\n"
"ds_read_b64_tr_b16 v[102:103], v126 offset:24832\n"
"s_add_u32 m0, s43, 86016\n"
"s_nop 0\n"
"buffer_load_dwordx4 v113, s[8:11], s84 offen offset:0 lds\n"
"s_waitcnt lgkmcnt(11)\n"
"v_mfma_f32_32x32x16_bf16 v[32:47], v[228:231], v[84:87], v[32:47]\n"
"v_accvgpr_read_b32 v133, a100\n"
"ds_write_b32 v131, v133 offset:59136\n"
"ds_read_b64_tr_b16 v[104:105], v127 offset:24576\n"
"ds_read_b64_tr_b16 v[106:107], v127 offset:24832\n"
"s_waitcnt lgkmcnt(12)\n"
"v_mfma_f32_32x32x16_bf16 v[32:47], v[232:235], v[88:91], v[32:47]\n"
"ds_read_b64_tr_b16 v[108:109], v128 offset:24576\n"
"ds_read_b64_tr_b16 v[110:111], v128 offset:24832\n"
"v_lshlrev_b32 v143, 7, v1\n"
"s_bfm_b32 s44, s70, 0\n"
"s_and_b32 s44, s44, 10836\n"
"s_bcnt1_i32_b32 s44, s44\n"
"s_sub_u32 s44, s70, s44\n"
"s_mul_i32 s42, s69, 66560\n"
"s_mul_i32 s41, s44, 6656\n"
"s_add_u32 s42, s42, s41\n"
"s_mul_i32 s41, s83, 3328\n"
"s_add_u32 s42, s42, s41\n"
"s_lshr_b32 s44, 0x2a54, s70\n"
"s_bitcmp1_b32 s44, 0\n"
"s_cselect_b32 s42, 2130706432, s42\n"
"s_mov_b64 exec, 0x3ffffff\n"
"buffer_load_dword a197, v143, s[16:19], s42 offen\n"
"s_mov_b64 exec, -1\n"
"s_waitcnt lgkmcnt(12)\n"
"v_mfma_f32_32x32x16_bf16 v[32:47], v[236:239], v[92:95], v[32:47]\n"
"s_waitcnt vmcnt(7)\n"
"s_waitcnt lgkmcnt(0)\n"
"s_barrier\n"
"v_mfma_f32_32x32x16_bf16 a[100:115], a[116:119], v[96:99], 0\n"
"ds_read_b128 v[76:79], v132 offset:59136\n"
"v_add_u32 v134, 65536, v124\n"
"v_add_u32 v125, s91, v134\n"
"v_add_u32 v126, s92, v134\n"
"v_add_u32 v127, s93, v134\n"
"v_add_u32 v128, s94, v134\n"
"v_mfma_f32_32x32x16_bf16 a[100:115], a[116:119], v[100:103], a[100:115]\n"
"ds_read_b64_tr_b16 v[80:81], v125 offset:0\n"
"ds_read_b64_tr_b16 v[82:83], v125 offset:256\n"
"ds_read_b64_tr_b16 v[84:85], v126 offset:0\n"
"ds_read_b64_tr_b16 v[86:87], v126 offset:256\n"
"v_mfma_f32_32x32x16_bf16 a[100:115], a[116:119], v[104:107], a[100:115]\n"
"ds_read_b64_tr_b16 v[88:89], v127 offset:0\n"
"ds_read_b64_tr_b16 v[90:91], v127 offset:256\n"
"ds_read_b64_tr_b16 v[92:93], v128 offset:0\n"
"ds_read_b64_tr_b16 v[94:95], v128 offset:256\n"
"v_mfma_f32_32x32x16_bf16 a[100:115], a[116:119], v[108:111], a[100:115]\n"
"s_add_u32 m0, s43, 90112\n"
"s_nop 0\n"
"buffer_load_dwordx4 v113, s[8:11], s86 offen offset:0 lds\n"
"v_mfma_f32_32x32x16_bf16 v[32:47], v[240:243], v[96:99], v[32:47]\n"
"s_waitcnt lgkmcnt(8)\n"
"v_add_f32 v135, v76, v77\n"
"v_add_f32 v133, v78, v79\n"
"v_add_f32 v135, v135, v133\n"
"v_mfma_f32_32x32x16_bf16 v[32:47], v[244:247], v[100:103], v[32:47]\n"
"s_nop 2\n"
"v_accvgpr_read_b32 v134, a100\n"
"v_add_f32 v135, v135, v134\n"
"v_mfma_f32_32x32x16_bf16 v[32:47], v[248:251], v[104:107], v[32:47]\n"
"v_cvt_pk_bf16_f32 v133, v135, v135\n"
"v_lshlrev_b32 v133, 16, v133\n"
"v_sub_f32 v12, v135, v133\n"
"v_cvt_pk_bf16_f32 v12, v133, v12\n"
"v_mfma_f32_32x32x16_bf16 v[32:47], v[252:255], v[108:111], v[32:47]\n"
"s_nop 0\n"
"v_mfma_f32_32x32x16_bf16 v[32:47], v[116:119], v[12:15], v[32:47]\n"
"v_mov_b32 v76, s77\n"
"v_mov_b32 v77, s77\n"
"v_mov_b32 v78, s77\n"
"v_mov_b32 v79, s77\n"
"s_mov_b64 exec, s[58:59]\n"
"s_nop 6\n"
"v_pk_mul_f32 v[32:33], v[32:33], v[48:49]\n"
"v_cvt_pk_bf16_f32 v72, v32, v33\n"
"v_pk_mul_f32 v[34:35], v[34:35], v[50:51]\n"
"v_cvt_pk_bf16_f32 v73, v34, v35\n"
"v_pk_mul_f32 v[36:37], v[36:37], v[52:53]\n"
"v_cvt_pk_bf16_f32 v74, v36, v37\n"
"v_pk_mul_f32 v[38:39], v[38:39], v[54:55]\n"
"v_cvt_pk_bf16_f32 v75, v38, v39\n"
"s_mov_b64 exec, -1\n"
"ds_write_b128 v112, v[72:75] offset:8192\n"
"s_mov_b64 exec, s[58:59]\n"
"v_pk_mul_f32 v[40:41], v[40:41], v[56:57]\n"
"v_cvt_pk_bf16_f32 v76, v40, v41\n"
"v_pk_mul_f32 v[42:43], v[42:43], v[58:59]\n"
"v_cvt_pk_bf16_f32 v77, v42, v43\n"
"v_pk_mul_f32 v[44:45], v[44:45], v[60:61]\n"
"v_cvt_pk_bf16_f32 v78, v44, v45\n"
"v_pk_mul_f32 v[46:47], v[46:47], v[62:63]\n"
"v_cvt_pk_bf16_f32 v79, v46, v47\n"
"s_mov_b64 exec, -1\n"
"ds_write_b128 v112, v[76:79] offset:9216\n"
"s_waitcnt vmcnt(7)\n"
"s_waitcnt lgkmcnt(0)\n"
"s_barrier\n"
"ds_read_b128 v[16:19], v114 offset:0\n"
"ds_read_b128 v[32:35], v115 offset:8192\n"
"ds_read_b128 v[20:23], v114 offset:2048\n"
"ds_read_b128 v[36:39], v115 offset:10240\n"
"ds_read_b128 v[24:27], v114 offset:4096\n"
"ds_read_b128 v[40:43], v115 offset:12288\n"
"ds_read_b128 v[28:31], v114 offset:6144\n"
"ds_read_b128 v[44:47], v115 offset:14336\n"
"s_waitcnt lgkmcnt(6)\n"
"v_mfma_f32_16x16x32_bf16 v[64:67], v[16:19], v[32:35], 0\n"
"s_waitcnt lgkmcnt(4)\n"
"v_mfma_f32_16x16x32_bf16 v[64:67], v[20:23], v[36:39], v[64:67]\n"
"s_waitcnt lgkmcnt(2)\n"
"v_mfma_f32_16x16x32_bf16 v[64:67], v[24:27], v[40:43], v[64:67]\n"
"s_waitcnt lgkmcnt(0)\n"
"v_mfma_f32_16x16x32_bf16 v[64:67], v[28:31], v[44:47], v[64:67]\n"
"s_nop 7\n"
"v_cvt_pk_bf16_f32 v68, v64, v65\n"
"v_cvt_pk_bf16_f32 v69, v66, v67\n"
"s_nop 0\n"
"v_permlane32_swap_b32 v68, v70\n"
"v_permlane32_swap_b32 v69, v71\n"
"s_mov_b64 exec, s[52:53]\n"
"buffer_store_dwordx4 v[68:71], v132, s[24:27], s36 offen sc1\n"
"s_mov_b64 exec, -1\n"
"v_accvgpr_read_b32 v32, a127\n"
"v_add_u32 v33, 6144, v32\n"
"v_add_u32 v34, 12288, v32\n"
"v_add_u32 v35, 18432, v32\n"
"v_mfma_f32_32x32x16_bf16 a[100:115], a[116:119], v[80:83], 0\n"
"ds_read_b64_tr_b16 v[96:97], v125 offset:4096\n"
"ds_read_b64_tr_b16 v[98:99], v125 offset:4352\n"
"v_bfe_u32 v42, v1, 4, 1\n"
"v_lshrrev_b32 v43, 5, v1\n"
"v_mfma_f32_32x32x16_bf16 v[48:63], v[80:83], v[144:147], 0\n"
"ds_read_b64_tr_b16 v[100:101], v126 offset:4096\n"
"ds_read_b64_tr_b16 v[102:103], v126 offset:4352\n"
"s_add_u32 m0, s49, 0\n"
"s_add_u32 s76, s48, 0\n"
"buffer_load_dwordx4 v32, s[12:15], s76 offen offset:0 lds\n"
"v_and_b32 v40, 15, v1\n"
"v_lshl_add_u32 v41, v42, 5, v40\n"
"v_lshlrev_b32 v41, 4, v41\n"
"v_mfma_f32_32x32x16_bf16 v[48:63], v[84:87], v[148:151], v[48:63]\n"
"ds_read_b64_tr_b16 v[104:105], v127 offset:4096\n"
"ds_read_b64_tr_b16 v[106:107], v127 offset:4352\n"
"v_lshl_add_u32 v41, v43, 3, v41\n"
"s_lshl_b32 s75, s83, 10\n"
"s_lshl_b32 s73, s82, 8\n"
"v_mfma_f32_32x32x16_bf16 v[48:63], v[88:91], v[152:155], v[48:63]\n"
"ds_read_b64_tr_b16 v[108:109], v128 offset:4096\n"
"ds_read_b64_tr_b16 v[110:111], v128 offset:4352\n"
"buffer_load_dwordx4 v33, s[12:15], s76 offen offset:1024 lds\n"
"s_add_u32 s75, s75, s73\n"
"s_add_u32 s75, s75, 60160\n"
"v_add_u32 v37, s75, v41\n"
"v_mfma_f32_32x32x16_bf16 v[48:63], v[92:95], v[156:159], v[48:63]\n"
"buffer_load_dwordx4 v34, s[12:15], s76 offen offset:2048 lds\n"
"s_lshl_b32 s75, s70, 8\n"
"s_lshl_b32 s73, s68, 2\n"
"s_add_u32 s75, s75, s73\n"
"buffer_load_dwordx4 v35, s[12:15], s76 offen offset:3072 lds\n"
"s_waitcnt vmcnt(11)\n"
"s_barrier\n"
"s_waitcnt lgkmcnt(6)\n"
"v_mfma_f32_32x32x16_bf16 a[100:115], a[116:119], v[96:99], a[100:115]\n"
"ds_read_b64_tr_b16 v[80:81], v125 offset:8192\n"
"ds_read_b64_tr_b16 v[82:83], v125 offset:8448\n"
"s_add_u32 s75, s75, 4096\n"
"v_lshlrev_b32 v41, 4, v40\n"
"v_add_u32 v138, s75, v41\n"
"v_mfma_f32_32x32x16_bf16 v[48:63], v[96:99], v[160:163], v[48:63]\n"
"ds_read_b64_tr_b16 v[84:85], v126 offset:8192\n"
"ds_read_b64_tr_b16 v[86:87], v126 offset:8448\n"
"s_add_u32 m0, s49, 4096\n"
"s_add_u32 s76, s48, 28672\n"
"buffer_load_dwordx4 v32, s[12:15], s76 offen offset:0 lds\n"
"s_add_u32 s51, s51, 4096\n"
"s_lshl_b32 s51, s70, 13\n"
"s_lshl_b32 s73, s69, 9\n"
"s_waitcnt lgkmcnt(8)\n"
"v_mfma_f32_32x32x16_bf16 v[48:63], v[100:103], v[164:167], v[48:63]\n"
"ds_read_b64_tr_b16 v[88:89], v127 offset:8192\n"
"ds_read_b64_tr_b16 v[90:91], v127 offset:8448\n"
"s_add_u32 s51, s51, s73\n"
"s_lshl_b32 s73, s68, 7\n"
"s_add_u32 s51, s51, s73\n"
"s_waitcnt lgkmcnt(8)\n"
"v_mfma_f32_32x32x16_bf16 v[48:63], v[104:107], v[168:171], v[48:63]\n"
"ds_read_b64_tr_b16 v[92:93], v128 offset:8192\n"
"ds_read_b64_tr_b16 v[94:95], v128 offset:8448\n"
"buffer_load_dwordx4 v33, s[12:15], s76 offen offset:1024 lds\n"
"s_add_u32 s51, s51, 4964352\n"
"buffer_load_dword v139, v138, s[24:27], 0 offen sc1\n"
"v_and_b32 v40, 0x1f0, v132\n"
"s_waitcnt lgkmcnt(8)\n"
"v_mfma_f32_32x32x16_bf16 v[48:63], v[108:111], v[172:175], v[48:63]\n"
"buffer_load_dwordx4 v34, s[12:15], s76 offen offset:2048 lds\n"
"v_lshl_add_u32 v36, v43, 11, v40\n"
"v_and_b32 v40, 16, v132\n"
"s_waitcnt vmcnt(14)\n"
"s_barrier\n"
"s_waitcnt lgkmcnt(6)\n"
"v_mfma_f32_32x32x16_bf16 a[100:115], a[116:119], v[80:83], a[100:115]\n"
"ds_read_b64_tr_b16 v[96:97], v125 offset:12288\n"
"ds_read_b64_tr_b16 v[98:99], v125 offset:12544\n"
"v_mov_b32 v41, 0x3f80\n"
"v_lshlrev_b32 v40, v40, v41\n"
"v_bfe_u32 v41, v1, 1, 4\n"
"v_mfma_f32_32x32x16_bf16 v[48:63], v[80:83], v[176:179], v[48:63]\n"
"ds_read_b64_tr_b16 v[100:101], v126 offset:12288\n"
"ds_read_b64_tr_b16 v[102:103], v126 offset:12544\n"
"buffer_load_dwordx4 v35, s[12:15], s76 offen offset:3072 lds\n"
"v_cmp_eq_u32 vcc, 0, v41\n"
"s_nop 1\n"
"v_cndmask_b32 v44, 0, v40, vcc\n"
"v_cmp_eq_u32 vcc, 1, v41\n"
"s_nop 1\n"
"v_cndmask_b32 v45, 0, v40, vcc\n"
"v_cmp_eq_u32 vcc, 2, v41\n"
"s_nop 1\n"
"v_cndmask_b32 v46, 0, v40, vcc\n"
"s_waitcnt lgkmcnt(8)\n"
"v_mfma_f32_32x32x16_bf16 v[48:63], v[84:87], v[180:183], v[48:63]\n"
"ds_read_b64_tr_b16 v[104:105], v127 offset:12288\n"
"ds_read_b64_tr_b16 v[106:107], v127 offset:12544\n"
"v_cmp_eq_u32 vcc, 3, v41\n"
"s_nop 1\n"
"v_cndmask_b32 v47, 0, v40, vcc\n"
"v_mov_b32 v22, v136\n"
"v_mov_b32 v23, v136\n"
"s_waitcnt lgkmcnt(8)\n"
"v_mfma_f32_32x32x16_bf16 v[48:63], v[88:91], v[184:187], v[48:63]\n"
"ds_read_b64_tr_b16 v[108:109], v128 offset:12288\n"
"ds_read_b64_tr_b16 v[110:111], v128 offset:12544\n"
"s_add_u32 m0, s49, 8192\n"
"s_add_u32 s76, s48, 57344\n"
"buffer_load_dwordx4 v32, s[12:15], s76 offen offset:0 lds\n"
"s_mov_b32 s86, s69\n"
"s_mul_i32 s75, s71, 14\n"
"s_add_u32 s86, s86, s75\n"
"s_waitcnt lgkmcnt(8)\n"
"v_mfma_f32_32x32x16_bf16 v[48:63], v[92:95], v[188:191], v[48:63]\n"
"buffer_load_dwordx4 v33, s[12:15], s76 offen offset:1024 lds\n"
"s_min_u32 s75, s86, 27\n"
"s_lshl_b32 s75, s75, 7\n"
"s_lshl_b32 s73, s61, 8\n"
"buffer_load_dwordx4 v34, s[12:15], s76 offen offset:2048 lds\n"
"s_waitcnt vmcnt(17)\n"
"s_barrier\n"
"s_waitcnt lgkmcnt(6)\n"
"v_mfma_f32_32x32x16_bf16 a[100:115], a[116:119], v[96:99], a[100:115]\n"
"ds_read_b64_tr_b16 v[80:81], v125 offset:16384\n"
"ds_read_b64_tr_b16 v[82:83], v125 offset:16640\n"
"s_cmp_eq_u32 s72, 7\n"
"s_cselect_b32 s84, s75, s73\n"
"s_mul_i32 s75, s83, 114688\n"
"s_add_u32 s84, s84, s75\n"
"v_mfma_f32_32x32x16_bf16 v[48:63], v[96:99], v[192:195], v[48:63]\n"
"ds_read_b64_tr_b16 v[84:85], v126 offset:16384\n"
"ds_read_b64_tr_b16 v[86:87], v126 offset:16640\n"
"buffer_load_dwordx4 v35, s[12:15], s76 offen offset:3072 lds\n"
"v_lshrrev_b32 v41, 3, v1\n"
"v_mul_u32_u24 v41, 3584, v41\n"
"s_lshl_b32 s75, s82, 1\n"
"s_waitcnt lgkmcnt(8)\n"
"v_mfma_f32_32x32x16_bf16 v[48:63], v[100:103], v[196:199], v[48:63]\n"
"ds_read_b64_tr_b16 v[88:89], v127 offset:16384\n"
"ds_read_b64_tr_b16 v[90:91], v127 offset:16640\n"
"v_or_b32 v40, s75, v42\n"
"v_bfe_u32 v43, v1, 1, 2\n"
"v_xor_b32 v40, v40, v43\n"
"s_waitcnt lgkmcnt(8)\n"
"v_mfma_f32_32x32x16_bf16 v[48:63], v[104:107], v[200:203], v[48:63]\n"
"ds_read_b64_tr_b16 v[92:93], v128 offset:16384\n"
"ds_read_b64_tr_b16 v[94:95], v128 offset:16640\n"
"s_add_u32 m0, s49, 12288\n"
"s_add_u32 s76, s48, 86016\n"
"buffer_load_dwordx4 v32, s[12:15], s76 offen offset:0 lds\n"
"v_lshl_add_u32 v41, v40, 5, v41\n"
"v_and_b32 v40, 16, v132\n"
"v_add3_u32 v41, v41, v40, s84\n"
"s_waitcnt lgkmcnt(8)\n"
"v_mfma_f32_32x32x16_bf16 v[48:63], v[108:111], v[204:207], v[48:63]\n"
"buffer_load_dwordx4 v33, s[12:15], s76 offen offset:1024 lds\n"
"s_mul_i32 s35, s82, 114688\n"
"s_mul_i32 s75, s83, 28672\n"
"s_add_u32 s35, s35, s75\n"
"s_waitcnt vmcnt(18)\n"
"s_barrier\n"
"s_waitcnt lgkmcnt(6)\n"
"v_mfma_f32_32x32x16_bf16 a[100:115], a[116:119], v[80:83], a[100:115]\n"
"ds_read_b64_tr_b16 v[96:97], v125 offset:20480\n"
"ds_read_b64_tr_b16 v[98:99], v125 offset:20736\n"
"s_lshl_b32 s75, s72, 7\n"
"s_add_u32 s35, s35, s75\n"
"v_mfma_f32_32x32x16_bf16 v[48:63], v[80:83], v[208:211], v[48:63]\n"
"ds_read_b64_tr_b16 v[100:101], v126 offset:20480\n"
"ds_read_b64_tr_b16 v[102:103], v126 offset:20736\n"
"s_waitcnt vmcnt(8)\n"
"v_cmp_ne_u32 vcc, v137, v139\n"
"s_nop 0\n"
"s_cbranch_vccnz L_fs_17_%=\n"
"L_fd_18_%=:\n"
"s_waitcnt lgkmcnt(8)\n"
"v_mfma_f32_32x32x16_bf16 v[48:63], v[84:87], v[212:215], v[48:63]\n"
"ds_read_b64_tr_b16 v[104:105], v127 offset:20480\n"
"ds_read_b64_tr_b16 v[106:107], v127 offset:20736\n"
"s_waitcnt lgkmcnt(8)\n"
"v_mfma_f32_32x32x16_bf16 v[48:63], v[88:91], v[216:219], v[48:63]\n"
"ds_read_b64_tr_b16 v[108:109], v128 offset:20480\n"
"ds_read_b64_tr_b16 v[110:111], v128 offset:20736\n"
"buffer_load_dwordx4 v[144:147], v36, s[24:27], s34 offen sc1\n"
"s_waitcnt lgkmcnt(8)\n"
"v_mfma_f32_32x32x16_bf16 v[48:63], v[92:95], v[220:223], v[48:63]\n"
"s_add_u32 s41, s34, 4096\n"
"buffer_load_dwordx4 v[148:151], v36, s[24:27], s41 offen sc1\n"
"s_add_u32 s41, s34, 8192\n"
"buffer_load_dwordx4 v[152:155], v36, s[24:27], s41 offen sc1\n"
"s_add_u32 s41, s34, 12288\n"
"buffer_load_dwordx4 v[156:159], v36, s[24:27], s41 offen sc1\n"
"s_barrier\n"
"s_waitcnt lgkmcnt(6)\n"
"v_mfma_f32_32x32x16_bf16 a[100:115], a[116:119], v[96:99], a[100:115]\n"
"ds_read_b64_tr_b16 v[80:81], v125 offset:24576\n"
"ds_read_b64_tr_b16 v[82:83], v125 offset:24832\n"
"v_mfma_f32_32x32x16_bf16 v[48:63], v[96:99], v[224:227], v[48:63]\n"
"ds_read_b64_tr_b16 v[84:85], v126 offset:24576\n"
"ds_read_b64_tr_b16 v[86:87], v126 offset:24832\n"
"s_add_u32 s41, s34, 16384\n"
"buffer_load_dwordx4 v[160:163], v36, s[24:27], s41 offen sc1\n"
"s_waitcnt lgkmcnt(8)\n"
"v_mfma_f32_32x32x16_bf16 v[48:63], v[100:103], v[228:231], v[48:63]\n"
"s_nop 2\n"
"v_accvgpr_read_b32 v133, a100\n"
"ds_write_b32 v131, v133 offset:59136\n"
"ds_read_b64_tr_b16 v[88:89], v127 offset:24576\n"
"ds_read_b64_tr_b16 v[90:91], v127 offset:24832\n"
"s_waitcnt lgkmcnt(9)\n"
"v_mfma_f32_32x32x16_bf16 v[48:63], v[104:107], v[232:235], v[48:63]\n"
"ds_read_b64_tr_b16 v[92:93], v128 offset:24576\n"
"ds_read_b64_tr_b16 v[94:95], v128 offset:24832\n"
"s_add_u32 s41, s34, 20480\n"
"buffer_load_dwordx4 v[164:167], v36, s[24:27], s41 offen sc1\n"
"s_waitcnt lgkmcnt(9)\n"
"v_mfma_f32_32x32x16_bf16 v[48:63], v[108:111], v[236:239], v[48:63]\n"
"s_add_u32 s41, s34, 24576\n"
"buffer_load_dwordx4 v[168:171], v36, s[24:27], s41 offen sc1\n"
"s_add_u32 s41, s34, 28672\n"
"buffer_load_dwordx4 v[172:175], v36, s[24:27], s41 offen sc1\n"
"s_waitcnt lgkmcnt(0)\n"
"s_barrier\n"
"v_mfma_f32_32x32x16_bf16 a[100:115], a[116:119], v[80:83], 0\n"
"ds_read_b128 v[76:79], v132 offset:59136\n"
"v_mfma_f32_32x32x16_bf16 a[100:115], a[116:119], v[84:87], a[100:115]\n"
"v_mfma_f32_32x32x16_bf16 a[100:115], a[116:119], v[88:91], a[100:115]\n"
"v_mfma_f32_32x32x16_bf16 a[100:115], a[116:119], v[92:95], a[100:115]\n"
"v_and_b32 v224, 0xf0, v132\n"
"v_lshlrev_b32 v224, 9, v224\n"
"s_lshl_b32 s51, s69, 9\n"
"s_lshl_b32 s42, s71, 8\n"
"s_add_u32 s51, s51, s42\n"
"s_lshl_b32 s42, s83, 7\n"
"s_add_u32 s51, s51, s42\n"
"s_add_u32 s51, s51, 4964352\n"
"buffer_load_dword v226, v224, s[24:27], s51 offen sc1\n"
"v_mfma_f32_32x32x16_bf16 v[48:63], v[80:83], v[240:243], v[48:63]\n"
"s_waitcnt lgkmcnt(0)\n"
"v_add_f32 v135, v76, v77\n"
"v_add_f32 v133, v78, v79\n"
"v_add_f32 v135, v135, v133\n"
"v_lshlrev_b32 v143, 7, v1\n"
"s_bfm_b32 s44, s70, 0\n"
"s_and_b32 s44, s44, 10836\n"
"s_bcnt1_i32_b32 s44, s44\n"
"s_sub_u32 s44, s70, s44\n"
"s_mul_i32 s42, s69, 35840\n"
"s_mul_i32 s41, s44, 3584\n"
"s_add_u32 s42, s42, s41\n"
"s_mul_i32 s41, s83, 1792\n"
"s_add_u32 s42, s42, s41\n"
"s_add_u32 s42, s42, 1064960\n"
"s_lshr_b32 s44, 0x2a54, s70\n"
"s_bitcmp1_b32 s44, 0\n"
"s_cselect_b32 s42, 2130706432, s42\n"
"s_mov_b64 exec, 0x3fff\n"
"buffer_load_dword a198, v143, s[16:19], s42 offen\n"
"s_mov_b64 exec, -1\n"
"v_mfma_f32_32x32x16_bf16 v[48:63], v[84:87], v[244:247], v[48:63]\n"
"v_accvgpr_read_b32 v134, a100\n"
"v_add_f32 v135, v135, v134\n"
"v_mfma_f32_32x32x16_bf16 v[48:63], v[88:91], v[248:251], v[48:63]\n"
"v_cvt_pk_bf16_f32 v133, v135, v135\n"
"v_lshlrev_b32 v133, 16, v133\n"
"v_sub_f32 v12, v135, v133\n"
"v_cvt_pk_bf16_f32 v12, v133, v12\n"
"v_mfma_f32_32x32x16_bf16 v[48:63], v[92:95], v[252:255], v[48:63]\n"
"v_lshlrev_b32 v143, 7, v1\n"
"s_bfm_b32 s44, s70, 0\n"
"s_and_b32 s44, s44, 10836\n"
"s_bcnt1_i32_b32 s44, s44\n"
"s_sub_u32 s44, s70, s44\n"
"s_mul_i32 s42, s69, 102400\n"
"s_mul_i32 s41, s44, 10240\n"
"s_add_u32 s42, s42, s41\n"
"s_mul_i32 s41, s68, 2560\n"
"s_add_u32 s42, s42, s41\n"
"s_lshr_b32 s44, 0x2a54, s70\n"
"s_bitcmp1_b32 s44, 0\n"
"s_cselect_b32 s42, 2130706432, s42\n"
"s_mov_b64 exec, 0xfffff\n"
"buffer_load_dword a199, v143, s[20:23], s42 offen\n"
"s_mov_b64 exec, -1\n"
"v_mfma_f32_32x32x16_bf16 v[48:63], v[12:15], v[116:119], v[48:63]\n"
"s_nop 11\n"
"v_pk_fma_f32 v[48:49], v[48:49], v[22:23], s[46:47]\n"
"v_exp_f32_e64 v64, v48 clamp\n"
"v_exp_f32_e64 v65, v49 clamp\n"
"v_max_f32 v48, s46, v48\n"
"v_max_f32 v49, s46, v49\n"
"v_pk_fma_f32 v[64:65], v[48:49], s[62:63], v[64:65]\n"
"v_cvt_pk_bf16_f32 v80, v64, v65\n"
"v_pk_fma_f32 v[50:51], v[50:51], v[22:23], s[46:47]\n"
"v_exp_f32_e64 v66, v50 clamp\n"
"v_exp_f32_e64 v67, v51 clamp\n"
"v_max_f32 v50, s46, v50\n"
"v_max_f32 v51, s46, v51\n"
"v_pk_fma_f32 v[66:67], v[50:51], s[62:63], v[66:67]\n"
"v_cvt_pk_bf16_f32 v81, v66, v67\n"
"v_pk_fma_f32 v[52:53], v[52:53], v[22:23], s[46:47]\n"
"v_exp_f32_e64 v64, v52 clamp\n"
"v_exp_f32_e64 v65, v53 clamp\n"
"v_max_f32 v52, s46, v52\n"
"v_max_f32 v53, s46, v53\n"
"v_pk_fma_f32 v[64:65], v[52:53], s[62:63], v[64:65]\n"
"v_cvt_pk_bf16_f32 v82, v64, v65\n"
"v_pk_fma_f32 v[54:55], v[54:55], v[22:23], s[46:47]\n"
"v_exp_f32_e64 v66, v54 clamp\n"
"v_exp_f32_e64 v67, v55 clamp\n"
"v_max_f32 v54, s46, v54\n"
"v_max_f32 v55, s46, v55\n"
"v_pk_fma_f32 v[66:67], v[54:55], s[62:63], v[66:67]\n"
"v_cvt_pk_bf16_f32 v83, v66, v67\n"
"v_pk_fma_f32 v[56:57], v[56:57], v[22:23], s[46:47]\n"
"v_exp_f32_e64 v64, v56 clamp\n"
"v_exp_f32_e64 v65, v57 clamp\n"
"v_max_f32 v56, s46, v56\n"
"v_max_f32 v57, s46, v57\n"
"v_pk_fma_f32 v[64:65], v[56:57], s[62:63], v[64:65]\n"
"v_cvt_pk_bf16_f32 v84, v64, v65\n"
"v_pk_fma_f32 v[58:59], v[58:59], v[22:23], s[46:47]\n"
"v_exp_f32_e64 v66, v58 clamp\n"
"v_exp_f32_e64 v67, v59 clamp\n"
"v_max_f32 v58, s46, v58\n"
"v_max_f32 v59, s46, v59\n"
"v_pk_fma_f32 v[66:67], v[58:59], s[62:63], v[66:67]\n"
"v_cvt_pk_bf16_f32 v85, v66, v67\n"
"v_pk_fma_f32 v[60:61], v[60:61], v[22:23], s[46:47]\n"
"v_exp_f32_e64 v64, v60 clamp\n"
"v_exp_f32_e64 v65, v61 clamp\n"
"v_max_f32 v60, s46, v60\n"
"v_max_f32 v61, s46, v61\n"
"v_pk_fma_f32 v[64:65], v[60:61], s[62:63], v[64:65]\n"
"v_cvt_pk_bf16_f32 v86, v64, v65\n"
"v_pk_fma_f32 v[62:63], v[62:63], v[22:23], s[46:47]\n"
"v_exp_f32_e64 v66, v62 clamp\n"
"v_exp_f32_e64 v67, v63 clamp\n"
"v_max_f32 v62, s46, v62\n"
"v_max_f32 v63, s46, v63\n"
"v_pk_fma_f32 v[66:67], v[62:63], s[62:63], v[66:67]\n"
"v_cvt_pk_bf16_f32 v87, v66, v67\n"
"s_mov_b64 exec, s[54:55]\n"
"v_mov_b32 v86, 0\n"
"v_mov_b32 v87, 0\n"
"s_mov_b64 exec, -1\n"
"v_add_u32 v3, 60160, v132\n"
"v_add_u32 v12, 0, v41\n"
"v_add_u32 v13, 27648, v41\n"
"v_add_u32 v14, 55296, v41\n"
"v_add_u32 v15, 82944, v41\n"
"s_mov_b32 s79, 20000\n"
"L_gather_15_%=:\n"
"s_waitcnt vmcnt(10)\n"
"v_mfma_f32_32x32x16_bf16 v[16:31], v[44:47], v[144:147], 0\n"
"v_max3_u32 v72, v144, v145, v146\n"
"v_max_u32 v72, v72, v147\n"
"s_waitcnt vmcnt(9)\n"
"v_mfma_f32_32x32x16_bf16 v[16:31], v[44:47], v[148:151], v[16:31]\n"
"v_max3_u32 v72, v72, v148, v149\n"
"v_max3_u32 v72, v72, v150, v151\n"
"s_waitcnt vmcnt(8)\n"
"v_mfma_f32_32x32x16_bf16 v[16:31], v[44:47], v[152:155], v[16:31]\n"
"v_max3_u32 v72, v72, v152, v153\n"
"v_max3_u32 v72, v72, v154, v155\n"
"s_waitcnt vmcnt(7)\n"
"v_mfma_f32_32x32x16_bf16 v[16:31], v[44:47], v[156:159], v[16:31]\n"
"v_max3_u32 v72, v72, v156, v157\n"
"v_max3_u32 v72, v72, v158, v159\n"
"s_waitcnt vmcnt(6)\n"
"v_mfma_f32_32x32x16_bf16 v[16:31], v[44:47], v[160:163], v[16:31]\n"
"v_max3_u32 v72, v72, v160, v161\n"
"v_max3_u32 v72, v72, v162, v163\n"
"s_waitcnt vmcnt(5)\n"
"v_mfma_f32_32x32x16_bf16 v[16:31], v[44:47], v[164:167], v[16:31]\n"
"v_max3_u32 v72, v72, v164, v165\n"
"v_max3_u32 v72, v72, v166, v167\n"
"s_waitcnt vmcnt(4)\n"
"v_mfma_f32_32x32x16_bf16 v[16:31], v[44:47], v[168:171], v[16:31]\n"
"v_max3_u32 v72, v72, v168, v169\n"
"v_max3_u32 v72, v72, v170, v171\n"
"s_waitcnt vmcnt(3)\n"
"v_mfma_f32_32x32x16_bf16 v[16:31], v[44:47], v[172:175], v[16:31]\n"
"v_max3_u32 v72, v72, v172, v173\n"
"v_max3_u32 v72, v72, v174, v175\n"
"v_cmp_eq_u32 vcc, -1, v72\n"
"s_nop 0\n"
"s_cbranch_vccnz L_gretry_19_%=\n"
"L_gdone_16_%=:\n"
"s_nop 6\n"
"v_cvt_pk_bf16_f32 v20, v16, v17\n"
"v_cvt_pk_bf16_f32 v21, v18, v19\n"
"ds_write_b64 v37, v[20:21]\n"
"s_waitcnt lgkmcnt(0)\n"
"s_barrier\n"
"ds_read_b128 v[144:147], v3\n"
"ds_read_b128 v[148:151], v3 offset:1024\n"
"s_waitcnt lgkmcnt(1)\n"
"v_mfma_f32_32x32x16_bf16 v[16:31], v[144:147], v[80:83], 0\n"
"s_waitcnt lgkmcnt(0)\n"
"v_mfma_f32_32x32x16_bf16 v[16:31], v[148:151], v[84:87], v[16:31]\n"
"s_nop 11\n"
"v_add_f32 v48, s64, v23\n"
"v_rcp_f32 v48, v48\n"
"v_mov_b32 v23, 0\n"
"v_mov_b32 v31, 0\n"
"v_mov_b32 v49, v48\n"
"v_pk_mul_f32 v[16:17], v[16:17], v[48:49]\n"
"v_cvt_pk_bf16_f32 v52, v16, v17\n"
"v_pk_mul_f32 v[18:19], v[18:19], v[48:49]\n"
"v_cvt_pk_bf16_f32 v53, v18, v19\n"
"v_pk_mul_f32 v[20:21], v[20:21], v[48:49]\n"
"v_cvt_pk_bf16_f32 v54, v20, v21\n"
"v_pk_mul_f32 v[22:23], v[22:23], v[48:49]\n"
"v_cvt_pk_bf16_f32 v55, v22, v23\n"
"buffer_store_dwordx4 v[52:55], v132, s[24:27], s37 offen offset:0 sc1\n"
"v_pk_mul_f32 v[24:25], v[24:25], v[48:49]\n"
"v_cvt_pk_bf16_f32 v56, v24, v25\n"
"v_pk_mul_f32 v[26:27], v[26:27], v[48:49]\n"
"v_cvt_pk_bf16_f32 v57, v26, v27\n"
"v_pk_mul_f32 v[28:29], v[28:29], v[48:49]\n"
"v_cvt_pk_bf16_f32 v58, v28, v29\n"
"v_pk_mul_f32 v[30:31], v[30:31], v[48:49]\n"
"v_cvt_pk_bf16_f32 v59, v30, v31\n"
"buffer_store_dwordx4 v[56:59], v132, s[24:27], s37 offen offset:1024 sc1\n"
"v_mov_b32 v225, v137\n"
"v_mov_b32 v227, v132\n"
"s_mov_b32 s78, 0\n"
"s_add_u32 m0, s85, 131072\n"
"s_nop 0\n"
"buffer_load_dwordx4 v12, s[16:19], s78 offen offset:0 lds\n"
"buffer_load_dwordx4 v13, s[16:19], s78 offen offset:1024 lds\n"
"buffer_load_dwordx4 v14, s[16:19], s78 offen offset:2048 lds\n"
"buffer_load_dwordx4 v15, s[16:19], s78 offen offset:3072 lds\n"
"s_mov_b32 s78, 229376\n"
"s_add_u32 m0, s85, 65536\n"
"s_nop 0\n"
"buffer_load_dwordx4 v12, s[16:19], s78 offen offset:0 lds\n"
"buffer_load_dwordx4 v13, s[16:19], s78 offen offset:1024 lds\n"
"buffer_load_dwordx4 v14, s[16:19], s78 offen offset:2048 lds\n"
"buffer_load_dwordx4 v15, s[16:19], s78 offen offset:3072 lds\n"
"v_and_b32 v2, 1, v1\n"
"v_bfe_u32 v3, v1, 1, 1\n"
"v_lshl_add_u32 v2, v2, 1, v3\n"
"v_lshlrev_b32 v2, 3, v2\n"
"v_bfe_u32 v206, v1, 2, 2\n"
"v_lshrrev_b32 v207, 5, v1\n"
"v_lshl_add_u32 v140, v207, 3, v206\n"
"v_mad_u32_u24 v141, v207, 7, v206\n"
"v_lshl_add_u32 v140, v140, 7, v2\n"
"v_bfe_u32 v3, v1, 4, 1\n"
"v_lshl_add_u32 v206, v3, 13, v140\n"
"v_bfe_u32 v207, v1, 3, 1\n"
"v_lshl_add_u32 v208, v3, 1, v207\n"
"v_xor_b32 v209, 0, v208\n"
"v_lshl_add_u32 v176, v209, 5, v206\n"
"v_add_u32 v180, 65536, v176\n"
"v_add_u32 v184, 131072, v176\n"
"v_xor_b32 v209, 1, v208\n"
"v_lshl_add_u32 v177, v209, 5, v206\n"
"v_add_u32 v181, 65536, v177\n"
"v_add_u32 v185, 131072, v177\n"
"v_xor_b32 v209, 2, v208\n"
"v_lshl_add_u32 v178, v209, 5, v206\n"
"v_add_u32 v182, 65536, v178\n"
"v_add_u32 v186, 131072, v178\n"
"v_xor_b32 v209, 3, v208\n"
"v_lshl_add_u32 v179, v209, 5, v206\n"
"v_add_u32 v183, 65536, v179\n"
"v_add_u32 v187, 131072, v179\n"
"s_cmp_eq_u32 s72, 7\n"
"s_cbranch_scc1 L_helper_20_%=\n"
"v_lshl_add_u32 v206, v3, 5, v140\n"
"v_xor_b32 v208, s82, v207\n"
"s_lshl_b32 s75, s82, 13\n"
"v_xor_b32 v209, 0, v208\n"
"v_lshl_add_u32 v209, v209, 6, v206\n"
"v_add_u32 v188, s75, v209\n"
"v_add_u32 v190, 65536, v188\n"
"v_add_u32 v192, 131072, v188\n"
"v_xor_b32 v209, 1, v208\n"
"v_lshl_add_u32 v209, v209, 6, v206\n"
"v_add_u32 v189, s75, v209\n"
"v_add_u32 v191, 65536, v189\n"
"v_add_u32 v193, 131072, v189\n"
"v_lshl_add_u32 v206, v3, 5, v2\n"
"s_mul_i32 s75, s82, 224\n"
"v_add_u32 v207, s75, v141\n"
"v_add_u32 v208, 0, v207\n"
"v_bfe_u32 v209, v208, 1, 1\n"
"v_xor_b32 v209, s82, v209\n"
"v_lshl_add_u32 v208, v208, 7, v206\n"
"v_xor_b32 v210, 0, v209\n"
"v_lshl_add_u32 v198, v210, 6, v208\n"
"v_xor_b32 v210, 1, v209\n"
"v_lshl_add_u32 v200, v210, 6, v208\n"
"v_add_u32 v208, 14, v207\n"
"v_bfe_u32 v209, v208, 1, 1\n"
"v_xor_b32 v209, s82, v209\n"
"v_lshl_add_u32 v208, v208, 7, v206\n"
"v_xor_b32 v210, 0, v209\n"
"v_lshl_add_u32 v199, v210, 6, v208\n"
"v_xor_b32 v210, 1, v209\n"
"v_lshl_add_u32 v201, v210, 6, v208\n"
"v_mov_b32 v194, v32\n"
"v_mov_b32 v195, v33\n"
"v_mov_b32 v196, v34\n"
"v_mov_b32 v197, v35\n"
"s_add_u32 s75, s85, 147456\n"
"v_add_u32 v218, s75, v227\n"
"s_xor_b32 s75, s85, 8192\n"
"s_add_u32 s75, s75, 147456\n"
"v_add_u32 v219, s75, v227\n"
"s_lshl_b32 s41, s81, 15\n"
"s_lshl_b32 s75, s82, 14\n"
"s_add_u32 s41, s41, s75\n"
"s_add_u32 s41, s41, 786432\n"
"s_add_u32 s42, s41, 4096\n"
"s_add_u32 s43, s41, 8192\n"
"s_add_u32 s44, s41, 12288\n"
"ds_read_b64_tr_b16 a[80:81], v200 offset:0\n"
"ds_read_b64_tr_b16 a[82:83], v200 offset:512\n"
"ds_read_b64_tr_b16 a[84:85], v198 offset:0\n"
"ds_read_b64_tr_b16 a[86:87], v198 offset:512\n"
"ds_read_b64_tr_b16 a[88:89], v201 offset:0\n"
"ds_read_b64_tr_b16 a[90:91], v201 offset:512\n"
"ds_read_b64_tr_b16 a[92:93], v199 offset:0\n"
"ds_read_b64_tr_b16 a[94:95], v199 offset:512\n"
"ds_read_b64_tr_b16 a[96:97], v200 offset:3584\n"
"ds_read_b64_tr_b16 a[98:99], v200 offset:4096\n"
"ds_read_b64_tr_b16 a[100:101], v198 offset:3584\n"
"ds_read_b64_tr_b16 a[102:103], v198 offset:4096\n"
"ds_read_b64_tr_b16 a[104:105], v201 offset:3584\n"
"ds_read_b64_tr_b16 a[106:107], v201 offset:4096\n"
"v_lshlrev_b32 v48, 16, v4\n"
"v_and_b32 v49, 0xffff0000, v4\n"
"v_lshlrev_b32 v50, 16, v5\n"
"v_and_b32 v51, 0xffff0000, v5\n"
"v_lshlrev_b32 v52, 16, v6\n"
"v_and_b32 v53, 0xffff0000, v6\n"
"v_lshlrev_b32 v54, 16, v7\n"
"v_and_b32 v55, 0xffff0000, v7\n"
"v_lshlrev_b32 v56, 16, v8\n"
"v_and_b32 v57, 0xffff0000, v8\n"
"v_lshlrev_b32 v58, 16, v9\n"
"v_and_b32 v59, 0xffff0000, v9\n"
"v_lshlrev_b32 v60, 16, v10\n"
"v_and_b32 v61, 0xffff0000, v10\n"
"v_lshlrev_b32 v62, 16, v11\n"
"v_and_b32 v63, 0xffff0000, v11\n"
"s_lshr_b32 s75, 0x2a54, s70\n"
"s_bitcmp1_b32 s75, 0\n"
"s_cbranch_scc1 L_nz_21_%=\n"
"s_sleep 7\n"
"L_nz_21_%=:\n"
"s_waitcnt vmcnt(12)\n"
"v_cmp_ne_u32 vcc, v225, v226\n"
"s_nop 0\n"
"s_cbranch_vccnz L_fs_22_%=\n"
"L_fd_23_%=:\n"
"buffer_load_dwordx4 v[80:83], v227, s[24:27], s41 offen offset:0 sc1\n"
"buffer_load_dwordx4 v[84:87], v227, s[24:27], s41 offen offset:1024 sc1\n"
"buffer_load_dwordx4 v[88:91], v227, s[24:27], s41 offen offset:2048 sc1\n"
"buffer_load_dwordx4 v[92:95], v227, s[24:27], s41 offen offset:3072 sc1\n"
"buffer_load_dwordx4 v[96:99], v227, s[24:27], s42 offen offset:0 sc1\n"
"buffer_load_dwordx4 v[100:103], v227, s[24:27], s42 offen offset:1024 sc1\n"
"buffer_load_dwordx4 v[104:107], v227, s[24:27], s42 offen offset:2048 sc1\n"
"buffer_load_dwordx4 v[108:111], v227, s[24:27], s42 offen offset:3072 sc1\n"
"buffer_load_dwordx4 v[112:115], v227, s[24:27], s43 offen offset:0 sc1\n"
"buffer_load_dwordx4 v[116:119], v227, s[24:27], s43 offen offset:1024 sc1\n"
"buffer_load_dwordx4 v[120:123], v227, s[24:27], s43 offen offset:2048 sc1\n"
"buffer_load_dwordx4 v[124:127], v227, s[24:27], s43 offen offset:3072 sc1\n"
"buffer_load_dwordx4 v[128:131], v227, s[24:27], s44 offen offset:0 sc1\n"
"buffer_load_dwordx4 v[132:135], v227, s[24:27], s44 offen offset:1024 sc1\n"
"buffer_load_dwordx4 v[136:139], v227, s[24:27], s44 offen offset:2048 sc1\n"
"buffer_load_dwordx4 v[140:143], v227, s[24:27], s44 offen offset:3072 sc1\n"
"s_waitcnt vmcnt(15)\n"
"v_max3_u32 v222, v80, v81, v82\n"
"s_waitcnt vmcnt(14)\n"
"v_max3_u32 v222, v222, v83, v84\n"
"v_max3_u32 v222, v222, v85, v86\n"
"v_max_u32 v222, v222, v87\n"
"v_cmp_eq_u32 vcc, -1, v222\n"
"s_mov_b32 s95, 0\n"
"s_cbranch_vccnz L_slowo_24_%=\n"
"L_ret_25_%=:\n"
"s_waitcnt lgkmcnt(0)\n"
"v_mfma_f32_32x32x16_bf16 v[32:47], a[80:83], v[80:83], 0\n"
"ds_read_b64_tr_b16 a[108:109], v199 offset:3584\n"
"ds_read_b64_tr_b16 a[110:111], v199 offset:4096\n"
"v_mfma_f32_32x32x16_bf16 v[16:31], a[84:87], v[80:83], v[48:63]\n"
"ds_read_b64_tr_b16 a[80:81], v200 offset:7168\n"
"ds_read_b64_tr_b16 a[82:83], v200 offset:7680\n"
"s_waitcnt vmcnt(13)\n"
"v_max3_u32 v222, v88, v89, v90\n"
"s_waitcnt vmcnt(12)\n"
"v_max3_u32 v222, v222, v91, v92\n"
"v_mfma_f32_32x32x16_bf16 v[32:47], a[88:91], v[84:87], v[32:47]\n"
"ds_read_b64_tr_b16 a[84:85], v198 offset:7168\n"
"ds_read_b64_tr_b16 a[86:87], v198 offset:7680\n"
"v_max3_u32 v222, v222, v93, v94\n"
"v_max_u32 v222, v222, v95\n"
"v_mfma_f32_32x32x16_bf16 v[16:31], a[92:95], v[84:87], v[16:31]\n"
"v_cmp_eq_u32 vcc, -1, v222\n"
"s_mov_b32 s95, 1\n"
"s_cbranch_vccnz L_slowo_24_%=\n"
"L_ret_26_%=:\n"
"ds_read_b64_tr_b16 a[88:89], v201 offset:7168\n"
"ds_read_b64_tr_b16 a[90:91], v201 offset:7680\n"
"v_mfma_f32_32x32x16_bf16 v[32:47], a[96:99], v[88:91], v[32:47]\n"
"ds_read_b64_tr_b16 a[92:93], v199 offset:7168\n"
"ds_read_b64_tr_b16 a[94:95], v199 offset:7680\n"
"v_mfma_f32_32x32x16_bf16 v[16:31], a[100:103], v[88:91], v[16:31]\n"
"ds_read_b64_tr_b16 a[96:97], v200 offset:10752\n"
"ds_read_b64_tr_b16 a[98:99], v200 offset:11264\n"
"s_waitcnt vmcnt(11)\n"
"v_max3_u32 v222, v96, v97, v98\n"
"s_waitcnt vmcnt(10)\n"
"v_max3_u32 v222, v222, v99, v100\n"
"v_mfma_f32_32x32x16_bf16 v[32:47], a[104:107], v[92:95], v[32:47]\n"
"ds_read_b64_tr_b16 a[100:101], v198 offset:10752\n"
"ds_read_b64_tr_b16 a[102:103], v198 offset:11264\n"
"v_max3_u32 v222, v222, v101, v102\n"
"v_max_u32 v222, v222, v103\n"
"s_waitcnt lgkmcnt(4)\n"
"v_mfma_f32_32x32x16_bf16 v[16:31], a[108:111], v[92:95], v[16:31]\n"
"v_cmp_eq_u32 vcc, -1, v222\n"
"s_mov_b32 s95, 2\n"
"s_cbranch_vccnz L_slowo_24_%=\n"
"L_ret_27_%=:\n"
"ds_read_b64_tr_b16 a[104:105], v201 offset:10752\n"
"ds_read_b64_tr_b16 a[106:107], v201 offset:11264\n"
"v_mfma_f32_32x32x16_bf16 v[32:47], a[80:83], v[96:99], v[32:47]\n"
"ds_read_b64_tr_b16 a[108:109], v199 offset:10752\n"
"ds_read_b64_tr_b16 a[110:111], v199 offset:11264\n"
"v_mfma_f32_32x32x16_bf16 v[16:31], a[84:87], v[96:99], v[16:31]\n"
"ds_read_b64_tr_b16 a[80:81], v200 offset:14336\n"
"ds_read_b64_tr_b16 a[82:83], v200 offset:14848\n"
"s_waitcnt vmcnt(9)\n"
"v_max3_u32 v222, v104, v105, v106\n"
"s_waitcnt vmcnt(8)\n"
"v_max3_u32 v222, v222, v107, v108\n"
"v_mfma_f32_32x32x16_bf16 v[32:47], a[88:91], v[100:103], v[32:47]\n"
"ds_read_b64_tr_b16 a[84:85], v198 offset:14336\n"
"ds_read_b64_tr_b16 a[86:87], v198 offset:14848\n"
"v_max3_u32 v222, v222, v109, v110\n"
"v_max_u32 v222, v222, v111\n"
"v_mfma_f32_32x32x16_bf16 v[16:31], a[92:95], v[100:103], v[16:31]\n"
"v_cmp_eq_u32 vcc, -1, v222\n"
"s_mov_b32 s95, 3\n"
"s_cbranch_vccnz L_slowo_24_%=\n"
"L_ret_28_%=:\n"
"ds_read_b64_tr_b16 a[88:89], v201 offset:14336\n"
"ds_read_b64_tr_b16 a[90:91], v201 offset:14848\n"
"s_waitcnt lgkmcnt(4)\n"
"v_mfma_f32_32x32x16_bf16 v[32:47], a[96:99], v[104:107], v[32:47]\n"
"s_mov_b32 s78, 458752\n"
"s_add_u32 m0, s85, 81920\n"
"ds_read_b64_tr_b16 a[92:93], v199 offset:14336\n"
"ds_read_b64_tr_b16 a[94:95], v199 offset:14848\n"
"v_mfma_f32_32x32x16_bf16 v[16:31], a[100:103], v[104:107], v[16:31]\n"
"buffer_load_dwordx4 v12, s[16:19], s78 offen offset:0 lds\n"
"v_mfma_f32_32x32x16_bf16 v[32:47], a[104:107], v[108:111], v[32:47]\n"
"buffer_load_dwordx4 v13, s[16:19], s78 offen offset:1024 lds\n"
"v_mfma_f32_32x32x16_bf16 v[16:31], a[108:111], v[108:111], v[16:31]\n"
"buffer_load_dwordx4 v14, s[16:19], s78 offen offset:2048 lds\n"
"s_waitcnt vmcnt(10)\n"
"v_max3_u32 v222, v112, v113, v114\n"
"s_waitcnt vmcnt(9)\n"
"v_max3_u32 v222, v222, v115, v116\n"
"v_max3_u32 v222, v222, v117, v118\n"
"v_max_u32 v222, v222, v119\n"
"v_cmp_eq_u32 vcc, -1, v222\n"
"s_mov_b32 s95, 4\n"
"s_cbranch_vccnz L_slowo_24_%=\n"
"L_ret_29_%=:\n"
"v_mfma_f32_32x32x16_bf16 v[32:47], a[80:83], v[112:115], v[32:47]\n"
"buffer_load_dwordx4 v15, s[16:19], s78 offen offset:3072 lds\n"
"s_waitcnt lgkmcnt(0)\n"
"v_mfma_f32_32x32x16_bf16 v[16:31], a[84:87], v[112:115], v[16:31]\n"
"ds_read_b64_tr_b16 a[96:97], v200 offset:17920\n"
"ds_read_b64_tr_b16 a[98:99], v200 offset:18432\n"
"s_mov_b32 s78, 688128\n"
"s_add_u32 m0, s85, 98304\n"
"ds_read_b64_tr_b16 a[100:101], v198 offset:17920\n"
"s_waitcnt vmcnt(9)\n"
"v_max3_u32 v222, v120, v121, v122\n"
"v_mfma_f32_32x32x16_bf16 v[32:47], a[88:91], v[116:119], v[32:47]\n"
"ds_read_b64_tr_b16 a[102:103], v198 offset:18432\n"
"buffer_load_dwordx4 v12, s[16:19], s78 offen offset:0 lds\n"
"v_mfma_f32_32x32x16_bf16 v[16:31], a[92:95], v[116:119], v[16:31]\n"
"ds_read_b64_tr_b16 a[104:105], v201 offset:17920\n"
"ds_read_b64_tr_b16 a[106:107], v201 offset:18432\n"
"ds_read_b64_tr_b16 a[108:109], v199 offset:17920\n"
"s_waitcnt vmcnt(9)\n"
"v_max3_u32 v222, v222, v123, v124\n"
"v_max3_u32 v222, v222, v125, v126\n"
"v_max_u32 v222, v222, v127\n"
"v_cmp_eq_u32 vcc, -1, v222\n"
"s_mov_b32 s95, 5\n"
"s_cbranch_vccnz L_slowo_24_%=\n"
"L_ret_30_%=:\n"
"s_waitcnt lgkmcnt(5)\n"
"v_mfma_f32_32x32x16_bf16 v[32:47], a[96:99], v[120:123], v[32:47]\n"
"ds_read_b64_tr_b16 a[110:111], v199 offset:18432\n"
"buffer_load_dwordx4 v13, s[16:19], s78 offen offset:1024 lds\n"
"s_waitcnt lgkmcnt(4)\n"
"v_mfma_f32_32x32x16_bf16 v[16:31], a[100:103], v[120:123], v[16:31]\n"
"ds_read_b64_tr_b16 a[80:81], v200 offset:21504\n"
"ds_read_b64_tr_b16 a[82:83], v200 offset:22016\n"
"ds_read_b64_tr_b16 a[84:85], v201 offset:21504\n"
"s_waitcnt vmcnt(9)\n"
"v_max3_u32 v222, v128, v129, v130\n"
"s_waitcnt lgkmcnt(5)\n"
"v_mfma_f32_32x32x16_bf16 v[32:47], a[104:107], v[124:127], v[32:47]\n"
"ds_read_b64_tr_b16 a[86:87], v201 offset:22016\n"
"buffer_load_dwordx4 v14, s[16:19], s78 offen offset:2048 lds\n"
"s_waitcnt lgkmcnt(4)\n"
"v_mfma_f32_32x32x16_bf16 v[16:31], a[108:111], v[124:127], v[16:31]\n"
"ds_read_b64_tr_b16 a[88:89], v200 offset:25088\n"
"ds_read_b64_tr_b16 a[90:91], v200 offset:25600\n"
"ds_read_b64_tr_b16 a[92:93], v201 offset:25088\n"
"s_waitcnt vmcnt(9)\n"
"v_max3_u32 v222, v222, v131, v132\n"
"v_max3_u32 v222, v222, v133, v134\n"
"v_max_u32 v222, v222, v135\n"
"v_cmp_eq_u32 vcc, -1, v222\n"
"s_mov_b32 s95, 6\n"
"s_cbranch_vccnz L_slowo_24_%=\n"
"L_ret_31_%=:\n"
"s_waitcnt lgkmcnt(5)\n"
"v_mfma_f32_32x32x16_bf16 v[32:47], a[80:83], v[128:131], v[32:47]\n"
"ds_read_b64_tr_b16 a[94:95], v201 offset:25600\n"
"buffer_load_dwordx4 v15, s[16:19], s78 offen offset:3072 lds\n"
"s_waitcnt lgkmcnt(4)\n"
"v_mfma_f32_32x32x16_bf16 v[32:47], a[84:87], v[132:135], v[32:47]\n"
"ds_read_b64_tr_b16 a[96:97], v198 offset:21504\n"
"ds_read_b64_tr_b16 a[98:99], v198 offset:22016\n"
"s_mov_b32 s78, 917504\n"
"s_add_u32 m0, s85, 114688\n"
"ds_read_b64_tr_b16 a[100:101], v199 offset:21504\n"
"s_waitcnt vmcnt(9)\n"
"v_max3_u32 v222, v136, v137, v138\n"
"s_waitcnt vmcnt(8)\n"
"v_max3_u32 v222, v222, v139, v140\n"
"v_max3_u32 v222, v222, v141, v142\n"
"v_max_u32 v222, v222, v143\n"
"v_cmp_eq_u32 vcc, -1, v222\n"
"s_mov_b32 s95, 7\n"
"s_cbranch_vccnz L_slowo_24_%=\n"
"L_ret_32_%=:\n"
"s_waitcnt lgkmcnt(5)\n"
"v_mfma_f32_32x32x16_bf16 v[32:47], a[88:91], v[136:139], v[32:47]\n"
"ds_read_b64_tr_b16 a[102:103], v199 offset:22016\n"
"buffer_load_dwordx4 v12, s[16:19], s78 offen offset:0 lds\n"
"s_waitcnt lgkmcnt(4)\n"
"v_mfma_f32_32x32x16_bf16 v[32:47], a[92:95], v[140:143], v[32:47]\n"
"ds_read_b64_tr_b16 a[104:105], v198 offset:25088\n"
"ds_read_b64_tr_b16 a[106:107], v198 offset:25600\n"
"ds_read_b64_tr_b16 a[108:109], v199 offset:25088\n"
"s_waitcnt lgkmcnt(5)\n"
"v_mfma_f32_32x32x16_bf16 v[16:31], a[96:99], v[128:131], v[16:31]\n"
"ds_read_b64_tr_b16 a[110:111], v199 offset:25600\n"
"buffer_load_dwordx4 v13, s[16:19], s78 offen offset:1024 lds\n"
"s_waitcnt lgkmcnt(4)\n"
"v_mfma_f32_32x32x16_bf16 v[16:31], a[100:103], v[132:135], v[16:31]\n"
"buffer_load_dwordx4 v14, s[16:19], s78 offen offset:2048 lds\n"
"s_waitcnt lgkmcnt(2)\n"
"v_mfma_f32_32x32x16_bf16 v[16:31], a[104:107], v[136:139], v[16:31]\n"
"ds_write_b128 v218, v[32:35] offset:0\n"
"ds_write_b128 v218, v[36:39] offset:1024\n"
"s_waitcnt lgkmcnt(2)\n"
"v_mfma_f32_32x32x16_bf16 v[16:31], a[108:111], v[140:143], v[16:31]\n"
"ds_write_b128 v218, v[40:43] offset:2048\n"
"ds_write_b128 v218, v[44:47] offset:3072\n"
"buffer_load_dwordx4 v15, s[16:19], s78 offen offset:3072 lds\n"
"v_lshlrev_b32 v204, 3, v1\n"
"v_mov_b32 v202, 0\n"
"v_mov_b32 v203, 0\n"
"s_waitcnt lgkmcnt(0)\n"
"s_barrier\n"
"ds_read_b128 v[32:35], v219 offset:0\n"
"ds_read_b128 v[36:39], v219 offset:1024\n"
"ds_read_b128 v[40:43], v219 offset:2048\n"
"ds_read_b128 v[44:47], v219 offset:3072\n"
"s_waitcnt lgkmcnt(3)\n"
"v_pk_add_f32 v[16:17], v[16:17], v[32:33]\n"
"v_cvt_pk_bf16_f32 v4, v16, v17\n"
"v_pk_add_f32 v[18:19], v[18:19], v[34:35]\n"
"v_cvt_pk_bf16_f32 v5, v18, v19\n"
"s_waitcnt lgkmcnt(2)\n"
"v_pk_add_f32 v[20:21], v[20:21], v[36:37]\n"
"v_cvt_pk_bf16_f32 v6, v20, v21\n"
"v_pk_add_f32 v[22:23], v[22:23], v[38:39]\n"
"v_cvt_pk_bf16_f32 v7, v22, v23\n"
"buffer_store_dwordx4 v[4:7], v227, s[24:27], s38 offen offset:0 sc1\n"
"s_waitcnt lgkmcnt(1)\n"
"v_pk_add_f32 v[24:25], v[24:25], v[40:41]\n"
"v_cvt_pk_bf16_f32 v8, v24, v25\n"
"v_pk_add_f32 v[26:27], v[26:27], v[42:43]\n"
"v_cvt_pk_bf16_f32 v9, v26, v27\n"
"s_waitcnt lgkmcnt(0)\n"
"v_pk_add_f32 v[28:29], v[28:29], v[44:45]\n"
"v_cvt_pk_bf16_f32 v10, v28, v29\n"
"v_pk_add_f32 v[30:31], v[30:31], v[46:47]\n"
"v_cvt_pk_bf16_f32 v11, v30, v31\n"
"buffer_store_dwordx4 v[8:11], v227, s[24:27], s38 offen offset:1024 sc1\n"
"v_dot2c_f32_bf16 v202, s77, v4\n"
"v_dot2c_f32_bf16 v203, v4, v4\n"
"v_dot2c_f32_bf16 v202, s77, v5\n"
"v_dot2c_f32_bf16 v203, v5, v5\n"
"v_dot2c_f32_bf16 v202, s77, v6\n"
"v_dot2c_f32_bf16 v203, v6, v6\n"
"v_dot2c_f32_bf16 v202, s77, v7\n"
"v_dot2c_f32_bf16 v203, v7, v7\n"
"v_dot2c_f32_bf16 v202, s77, v8\n"
"v_dot2c_f32_bf16 v203, v8, v8\n"
"v_dot2c_f32_bf16 v202, s77, v9\n"
"v_dot2c_f32_bf16 v203, v9, v9\n"
"v_dot2c_f32_bf16 v202, s77, v10\n"
"v_dot2c_f32_bf16 v203, v10, v10\n"
"v_dot2c_f32_bf16 v202, s77, v11\n"
"v_dot2c_f32_bf16 v203, v11, v11\n"
"s_nop 1\n"
"v_mov_b32 v205, v202\n"
"s_nop 1\n"
"v_permlane32_swap_b32 v205, v202\n"
"v_add_f32 v202, v202, v205\n"
"v_mov_b32 v206, v203\n"
"s_nop 1\n"
"v_permlane32_swap_b32 v206, v203\n"
"v_add_f32 v203, v203, v206\n"
"s_mov_b64 exec, s[52:53]\n"
"buffer_store_dwordx2 v[202:203], v204, s[24:27], s39 offen sc1\n"
"s_mov_b64 exec, -1\n"
"s_mov_b64 s[56:57], -1\n"
"L_c0_33_%=:\n"
"s_mov_b32 s78, 1146880\n"
"s_mov_b32 m0, s85\n"
"s_nop 0\n"
"buffer_load_dwordx4 v12, s[16:19], s78 offen offset:0 lds\n"
"buffer_load_dwordx4 v13, s[16:19], s78 offen offset:1024 lds\n"
"buffer_load_dwordx4 v14, s[16:19], s78 offen offset:2048 lds\n"
"buffer_load_dwordx4 v15, s[16:19], s78 offen offset:3072 lds\n"
"s_mov_b32 s78, 1376256\n"
"s_add_u32 m0, s85, 16384\n"
"s_nop 0\n"
"buffer_load_dwordx4 v12, s[16:19], s78 offen offset:0 lds\n"
"buffer_load_dwordx4 v13, s[16:19], s78 offen offset:1024 lds\n"
"buffer_load_dwordx4 v14, s[16:19], s78 offen offset:2048 lds\n"
"buffer_load_dwordx4 v15, s[16:19], s78 offen offset:3072 lds\n"
"s_mov_b32 s78, 128\n"
"s_add_u32 m0, s85, 32768\n"
"s_nop 0\n"
"buffer_load_dwordx4 v12, s[16:19], s78 offen offset:0 lds\n"
"buffer_load_dwordx4 v13, s[16:19], s78 offen offset:1024 lds\n"
"buffer_load_dwordx4 v14, s[16:19], s78 offen offset:2048 lds\n"
"buffer_load_dwordx4 v15, s[16:19], s78 offen offset:3072 lds\n"
"s_mul_i32 s41, s80, 28672\n"
"s_add_u32 s41, s41, 2883584\n"
"s_add_u32 s42, s41, 4096\n"
"s_add_u32 s43, s41, 8192\n"
"s_add_u32 s44, s41, 12288\n"
"s_add_u32 s45, s41, 16384\n"
"s_add_u32 s46, s41, 20480\n"
"s_add_u32 s47, s41, 24576\n"
"v_and_b32 v223, 31, v1\n"
"v_lshlrev_b32 v223, 3, v223\n"
"v_lshrrev_b32 v202, 5, v1\n"
"v_mul_u32_u24 v202, 1792, v202\n"
"v_add_u32 v223, v223, v202\n"
"s_mul_i32 s39, s80, 3584\n"
"s_add_u32 s39, s39, 4718592\n"
"v_and_b32 v224, 3, v1\n"
"v_bfe_u32 v202, v1, 2, 1\n"
"v_lshlrev_b32 v224, 2, v224\n"
"v_lshl_add_u32 v224, v202, 7, v224\n"
"s_lshl_b32 s50, s61, 8\n"
"s_add_u32 s50, s50, 16384\n"
"ds_read_b64_tr_b16 a[80:81], v184 offset:0\n"
"ds_read_b64_tr_b16 a[82:83], v184 offset:512\n"
"ds_read_b64_tr_b16 a[84:85], v185 offset:0\n"
"ds_read_b64_tr_b16 a[86:87], v185 offset:512\n"
"ds_read_b64_tr_b16 a[88:89], v186 offset:0\n"
"ds_read_b64_tr_b16 a[90:91], v186 offset:512\n"
"ds_read_b64_tr_b16 a[92:93], v187 offset:0\n"
"ds_read_b64_tr_b16 a[94:95], v187 offset:512\n"
"ds_read_b64_tr_b16 a[96:97], v184 offset:2048\n"
"ds_read_b64_tr_b16 a[98:99], v184 offset:2560\n"
"ds_read_b64_tr_b16 a[100:101], v185 offset:2048\n"
"ds_read_b64_tr_b16 a[102:103], v185 offset:2560\n"
"ds_read_b64_tr_b16 a[104:105], v186 offset:2048\n"
"ds_read_b64_tr_b16 a[106:107], v186 offset:2560\n"
"s_sleep 4\n"
"s_mov_b32 s79, 20000\n"
"L_htry_34_%=:\n"
"buffer_load_dwordx2 v[80:81], v223, s[24:27], s39 offen offset:0 sc1\n"
"buffer_load_dwordx2 v[82:83], v223, s[24:27], s39 offen offset:256 sc1\n"
"buffer_load_dwordx2 v[84:85], v223, s[24:27], s39 offen offset:512 sc1\n"
"buffer_load_dwordx2 v[86:87], v223, s[24:27], s39 offen offset:768 sc1\n"
"buffer_load_dwordx2 v[88:89], v223, s[24:27], s39 offen offset:1024 sc1\n"
"buffer_load_dwordx2 v[90:91], v223, s[24:27], s39 offen offset:1280 sc1\n"
"buffer_load_dwordx2 v[92:93], v223, s[24:27], s39 offen offset:1536 sc1\n"
"buffer_load_dwordx4 v[144:147], v227, s[24:27], s41 offen offset:0 sc1\n"
"buffer_load_dwordx4 v[148:151], v227, s[24:27], s41 offen offset:1024 sc1\n"
"buffer_load_dwordx4 v[152:155], v227, s[24:27], s41 offen offset:2048 sc1\n"
"buffer_load_dwordx4 v[156:159], v227, s[24:27], s41 offen offset:3072 sc1\n"
"buffer_load_dwordx4 v[160:163], v227, s[24:27], s42 offen offset:0 sc1\n"
"buffer_load_dwordx4 v[164:167], v227, s[24:27], s42 offen offset:1024 sc1\n"
"buffer_load_dwordx4 v[168:171], v227, s[24:27], s42 offen offset:2048 sc1\n"
"buffer_load_dwordx4 v[172:175], v227, s[24:27], s42 offen offset:3072 sc1\n"
"s_waitcnt vmcnt(13)\n"
"v_max3_u32 v222, v80, v81, v82\n"
"s_waitcnt vmcnt(12)\n"
"v_max3_u32 v222, v222, v83, v84\n"
"s_waitcnt vmcnt(11)\n"
"v_max3_u32 v222, v222, v85, v86\n"
"s_waitcnt vmcnt(10)\n"
"v_max3_u32 v222, v222, v87, v88\n"
"s_waitcnt vmcnt(9)\n"
"v_max3_u32 v222, v222, v89, v90\n"
"s_waitcnt vmcnt(8)\n"
"v_max3_u32 v222, v222, v91, v92\n"
"v_max_u32 v222, v222, v93\n"
"v_cmp_eq_u32 vcc, -1, v222\n"
"s_nop 0\n"
"s_cbranch_vccnz L_hslow_36_%=\n"
"L_hgo_35_%=:\n"
"s_waitcnt vmcnt(0)\n"
"s_waitcnt lgkmcnt(0)\n"
"v_mfma_f32_32x32x16_bf16 v[16:31], a[80:83], v[144:147], 0\n"
"buffer_load_dwordx4 a[0:3], v227, s[24:27], s43 offen offset:0 sc1\n"
"ds_read_b64_tr_b16 a[108:109], v187 offset:2048\n"
"ds_read_b64_tr_b16 a[110:111], v187 offset:2560\n"
"s_add_u32 s48, s40, 57344\n"
"v_mfma_f32_32x32x16_bf16 v[32:47], a[84:87], v[144:147], 0\n"
"buffer_load_dwordx4 a[4:7], v227, s[24:27], s43 offen offset:1024 sc1\n"
"ds_read_b64_tr_b16 a[80:81], v184 offset:4096\n"
"ds_read_b64_tr_b16 a[82:83], v184 offset:4608\n"
"s_lshl_b32 s36, s80, 3\n"
"v_mfma_f32_32x32x16_bf16 v[48:63], a[88:91], v[144:147], 0\n"
"buffer_load_dwordx4 a[8:11], v227, s[24:27], s43 offen offset:2048 sc1\n"
"ds_read_b64_tr_b16 a[84:85], v185 offset:4096\n"
"ds_read_b64_tr_b16 a[86:87], v185 offset:4608\n"
"s_lshl_b32 s76, s71, 2\n"
"v_mfma_f32_32x32x16_bf16 v[64:79], a[92:95], v[144:147], 0\n"
"buffer_load_dwordx4 a[12:15], v227, s[24:27], s43 offen offset:3072 sc1\n"
"ds_read_b64_tr_b16 a[88:89], v186 offset:4096\n"
"ds_read_b64_tr_b16 a[90:91], v186 offset:4608\n"
"s_add_u32 s36, s36, s76\n"
"v_mfma_f32_32x32x16_bf16 v[16:31], a[96:99], v[148:151], v[16:31]\n"
"buffer_load_dwordx4 a[16:19], v227, s[24:27], s44 offen offset:0 sc1\n"
"ds_read_b64_tr_b16 a[92:93], v187 offset:4096\n"
"ds_read_b64_tr_b16 a[94:95], v187 offset:4608\n"
"s_add_u32 s36, s36, 12288\n"
"v_mfma_f32_32x32x16_bf16 v[32:47], a[100:103], v[148:151], v[32:47]\n"
"s_mov_b32 s78, 229504\n"
"s_add_u32 m0, s85, 49152\n"
"ds_read_b64_tr_b16 a[96:97], v184 offset:6144\n"
"ds_read_b64_tr_b16 a[98:99], v184 offset:6656\n"
"v_add_f32 v80, v80, v82\n"
"v_add_f32 v81, v81, v83\n"
"v_mfma_f32_32x32x16_bf16 v[48:63], a[104:107], v[148:151], v[48:63]\n"
"buffer_load_dwordx4 a[20:23], v227, s[24:27], s44 offen offset:1024 sc1\n"
"ds_read_b64_tr_b16 a[100:101], v185 offset:6144\n"
"ds_read_b64_tr_b16 a[102:103], v185 offset:6656\n"
"s_waitcnt lgkmcnt(4)\n"
"v_mfma_f32_32x32x16_bf16 v[64:79], a[108:111], v[148:151], v[64:79]\n"
"ds_read_b64_tr_b16 a[104:105], v186 offset:6144\n"
"ds_read_b64_tr_b16 a[106:107], v186 offset:6656\n"
"v_add_f32 v80, v80, v84\n"
"v_add_f32 v81, v81, v85\n"
"v_add_f32 v80, v80, v86\n"
"v_add_f32 v81, v81, v87\n"
"v_mfma_f32_32x32x16_bf16 v[16:31], a[80:83], v[152:155], v[16:31]\n"
"buffer_load_dwordx4 a[24:27], v227, s[24:27], s44 offen offset:2048 sc1\n"
"ds_read_b64_tr_b16 a[108:109], v187 offset:6144\n"
"ds_read_b64_tr_b16 a[110:111], v187 offset:6656\n"
"v_add_f32 v80, v80, v88\n"
"v_mfma_f32_32x32x16_bf16 v[32:47], a[84:87], v[152:155], v[32:47]\n"
"ds_read_b64_tr_b16 a[80:81], v180 offset:0\n"
"ds_read_b64_tr_b16 a[82:83], v180 offset:512\n"
"v_add_f32 v81, v81, v89\n"
"v_add_f32 v80, v80, v90\n"
"v_add_f32 v81, v81, v91\n"
"v_add_f32 v80, v80, v92\n"
"v_mfma_f32_32x32x16_bf16 v[48:63], a[88:91], v[152:155], v[48:63]\n"
"buffer_load_dwordx4 a[28:31], v227, s[24:27], s44 offen offset:3072 sc1\n"
"ds_read_b64_tr_b16 a[84:85], v181 offset:0\n"
"ds_read_b64_tr_b16 a[86:87], v181 offset:512\n"
"v_add_f32 v81, v81, v93\n"
"v_mfma_f32_32x32x16_bf16 v[64:79], a[92:95], v[152:155], v[64:79]\n"
"buffer_load_dwordx4 a[32:35], v227, s[24:27], s45 offen offset:0 sc1\n"
"ds_read_b64_tr_b16 a[88:89], v182 offset:0\n"
"ds_read_b64_tr_b16 a[90:91], v182 offset:512\n"
"s_waitcnt lgkmcnt(4)\n"
"v_mfma_f32_32x32x16_bf16 v[16:31], a[96:99], v[156:159], v[16:31]\n"
"ds_read_b64_tr_b16 a[92:93], v183 offset:0\n"
"ds_read_b64_tr_b16 a[94:95], v183 offset:512\n"
"v_mov_b32 v82, v80\n"
"v_mov_b32 v83, v81\n"
"v_and_b32 v87, 31, v1\n"
"v_permlane32_swap_b32 v82, v80\n"
"v_mfma_f32_32x32x16_bf16 v[32:47], a[100:103], v[156:159], v[32:47]\n"
"buffer_load_dwordx4 a[36:39], v227, s[24:27], s45 offen offset:1024 sc1\n"
"ds_read_b64_tr_b16 a[96:97], v180 offset:2048\n"
"ds_read_b64_tr_b16 a[98:99], v180 offset:2560\n"
"v_permlane32_swap_b32 v83, v81\n"
"v_mfma_f32_32x32x16_bf16 v[48:63], a[104:107], v[156:159], v[48:63]\n"
"ds_read_b64_tr_b16 a[100:101], v181 offset:2048\n"
"ds_read_b64_tr_b16 a[102:103], v181 offset:2560\n"
"v_add_f32 v80, v80, v82\n"
"v_add_f32 v81, v81, v83\n"
"v_mul_f32 v80, 0xbb124925, v80\n"
"v_mul_f32 v81, 0x3b124925, v81\n"
"v_mfma_f32_32x32x16_bf16 v[64:79], a[108:111], v[156:159], v[64:79]\n"
"buffer_load_dwordx4 a[40:43], v227, s[24:27], s45 offen offset:2048 sc1\n"
"ds_read_b64_tr_b16 a[104:105], v182 offset:2048\n"
"ds_read_b64_tr_b16 a[106:107], v182 offset:2560\n"
"v_fma_f32 v81, -v80, v80, v81\n"
"s_barrier\n"
"v_mfma_f32_32x32x16_bf16 v[16:31], a[80:83], v[160:163], v[16:31]\n"
"ds_read_b64_tr_b16 a[108:109], v183 offset:2048\n"
"ds_read_b64_tr_b16 a[110:111], v183 offset:2560\n"
"v_max_f32 v81, 0, v81\n"
"v_add_f32 v81, 0x3727c5ac, v81\n"
"s_waitcnt lgkmcnt(4)\n"
"v_mfma_f32_32x32x16_bf16 v[32:47], a[84:87], v[160:163], v[32:47]\n"
"buffer_load_dwordx4 a[44:47], v227, s[24:27], s45 offen offset:3072 sc1\n"
"ds_read_b64_tr_b16 a[80:81], v180 offset:4096\n"
"ds_read_b64_tr_b16 a[82:83], v180 offset:4608\n"
"v_mfma_f32_32x32x16_bf16 v[48:63], a[88:91], v[160:163], v[48:63]\n"
"ds_read_b64_tr_b16 a[84:85], v181 offset:4096\n"
"ds_read_b64_tr_b16 a[86:87], v181 offset:4608\n"
"v_rcp_f32 v81, v81\n"
"v_lshlrev_b32 v82, 2, v87\n"
"s_lshl_b32 s75, s68, 7\n"
"v_mfma_f32_32x32x16_bf16 v[64:79], a[92:95], v[160:163], v[64:79]\n"
"buffer_load_dwordx4 a[48:51], v227, s[24:27], s46 offen offset:0 sc1\n"
"ds_read_b64_tr_b16 a[88:89], v182 offset:4096\n"
"ds_read_b64_tr_b16 a[90:91], v182 offset:4608\n"
"s_add_u32 s75, s75, 147456\n"
"v_mfma_f32_32x32x16_bf16 v[16:31], a[96:99], v[164:167], v[16:31]\n"
"buffer_load_dwordx4 a[52:55], v227, s[24:27], s46 offen offset:1024 sc1\n"
"ds_read_b64_tr_b16 a[92:93], v183 offset:4096\n"
"ds_read_b64_tr_b16 a[94:95], v183 offset:4608\n"
"v_add_u32 v83, s75, v82\n"
"v_mfma_f32_32x32x16_bf16 v[32:47], a[100:103], v[164:167], v[32:47]\n"
"ds_read_b64_tr_b16 a[96:97], v180 offset:6144\n"
"ds_read_b64_tr_b16 a[98:99], v180 offset:6656\n"
"ds_write_b32 v83, v81\n"
"v_cvt_pk_bf16_f32 v85, v80, v80\n"
"v_lshlrev_b32 v86, 16, v85\n"
"s_waitcnt lgkmcnt(5)\n"
"v_mfma_f32_32x32x16_bf16 v[48:63], a[104:107], v[164:167], v[48:63]\n"
"buffer_load_dwordx4 a[56:59], v227, s[24:27], s46 offen offset:2048 sc1\n"
"ds_read_b64_tr_b16 a[100:101], v181 offset:6144\n"
"ds_read_b64_tr_b16 a[102:103], v181 offset:6656\n"
"v_sub_f32 v86, v80, v86\n"
"v_mfma_f32_32x32x16_bf16 v[64:79], a[108:111], v[164:167], v[64:79]\n"
"ds_read_b64_tr_b16 a[104:105], v182 offset:6144\n"
"ds_read_b64_tr_b16 a[106:107], v182 offset:6656\n"
"v_cvt_pk_bf16_f32 v86, v86, 0\n"
"v_cndmask_b32 v84, v85, v86, s[54:55]\n"
"v_accvgpr_write_b32 a232, 0\n"
"v_accvgpr_write_b32 a233, 0\n"
"v_mfma_f32_32x32x16_bf16 v[16:31], a[80:83], v[168:171], v[16:31]\n"
"buffer_load_dwordx4 a[60:63], v227, s[24:27], s46 offen offset:3072 sc1\n"
"ds_read_b64_tr_b16 a[108:109], v183 offset:6144\n"
"ds_read_b64_tr_b16 a[110:111], v183 offset:6656\n"
"v_accvgpr_write_b32 a234, v84\n"
"v_mfma_f32_32x32x16_bf16 v[32:47], a[84:87], v[168:171], v[32:47]\n"
"ds_read_b64_tr_b16 a[80:81], v180 offset:16384\n"
"ds_read_b64_tr_b16 a[82:83], v180 offset:16896\n"
"v_accvgpr_write_b32 a235, 0\n"
"v_accvgpr_write_b32 a236, 0\n"
"v_accvgpr_write_b32 a237, 0\n"
"v_accvgpr_write_b32 a238, 0\n"
"v_mfma_f32_32x32x16_bf16 v[48:63], a[88:91], v[168:171], v[48:63]\n"
"buffer_load_dwordx4 a[64:67], v227, s[24:27], s47 offen offset:0 sc1\n"
"ds_read_b64_tr_b16 a[84:85], v181 offset:16384\n"
"v_accvgpr_write_b32 a239, 0\n"
"s_waitcnt lgkmcnt(3)\n"
"v_mfma_f32_32x32x16_bf16 v[64:79], a[92:95], v[168:171], v[64:79]\n"
"ds_read_b64_tr_b16 a[86:87], v181 offset:16896\n"
"ds_read_b64_tr_b16 a[88:89], v182 offset:16384\n"
"ds_read_b64_tr_b16 a[90:91], v182 offset:16896\n"
"v_accvgpr_write_b32 a240, 0\n"
"v_accvgpr_write_b32 a241, v84\n"
"v_accvgpr_write_b32 a242, 0\n"
"v_mfma_f32_32x32x16_bf16 v[16:31], a[96:99], v[172:175], v[16:31]\n"
"buffer_load_dwordx4 a[68:71], v227, s[24:27], s47 offen offset:1024 sc1\n"
"ds_read_b64_tr_b16 a[92:93], v183 offset:16384\n"
"ds_read_b64_tr_b16 a[94:95], v183 offset:16896\n"
"v_accvgpr_write_b32 a243, 0\n"
"v_mfma_f32_32x32x16_bf16 v[32:47], a[100:103], v[172:175], v[32:47]\n"
"buffer_load_dwordx4 a[72:75], v227, s[24:27], s47 offen offset:2048 sc1\n"
"ds_read_b64_tr_b16 a[96:97], v180 offset:18432\n"
"ds_read_b64_tr_b16 a[98:99], v180 offset:18944\n"
"s_lshl_b32 s75, s71, 8\n"
"v_mfma_f32_32x32x16_bf16 v[48:63], a[104:107], v[172:175], v[48:63]\n"
"ds_read_b64_tr_b16 a[100:101], v181 offset:18432\n"
"ds_read_b64_tr_b16 a[102:103], v181 offset:18944\n"
"s_lshl_b32 s76, s83, 7\n"
"s_add_u32 s75, s75, s76\n"
"s_add_u32 s75, s75, 147456\n"
"v_add_u32 v221, s75, v82\n"
"v_mfma_f32_32x32x16_bf16 v[64:79], a[108:111], v[172:175], v[64:79]\n"
"buffer_load_dwordx4 a[76:79], v227, s[24:27], s47 offen offset:3072 sc1\n"
"ds_read_b64_tr_b16 a[104:105], v182 offset:18432\n"
"ds_read_b64_tr_b16 a[106:107], v182 offset:18944\n"
"s_barrier\n"
"s_waitcnt vmcnt(15)\n"
"s_waitcnt lgkmcnt(4)\n"
"v_mfma_f32_32x32x16_bf16 v[16:31], a[80:83], a[0:3], v[16:31]\n"
"ds_read_b64_tr_b16 a[108:109], v183 offset:18432\n"
"ds_read_b64_tr_b16 a[110:111], v183 offset:18944\n"
"v_add_u32 v240, 148480, v227\n"
"s_lshl_b32 s75, s68, 2\n"
"v_add_u32 v241, s75, v240\n"
"v_add_u32 v242, s85, v227\n"
"v_mfma_f32_32x32x16_bf16 v[32:47], a[84:87], a[0:3], v[32:47]\n"
"buffer_load_dwordx4 v12, s[16:19], s78 offen offset:0 lds\n"
"v_mfma_f32_32x32x16_bf16 v[48:63], a[88:91], a[0:3], v[48:63]\n"
"ds_read_b64_tr_b16 a[80:81], v180 offset:20480\n"
"ds_read_b64_tr_b16 a[82:83], v180 offset:20992\n"
"ds_read_b64_tr_b16 a[84:85], v181 offset:20480\n"
"v_add_u32 v243, 65536, v242\n"
"v_mfma_f32_32x32x16_bf16 v[64:79], a[92:95], a[0:3], v[64:79]\n"
"buffer_load_dwordx4 v13, s[16:19], s78 offen offset:1024 lds\n"
"v_mfma_f32_32x32x16_bf16 v[16:31], a[96:99], a[4:7], v[16:31]\n"
"buffer_load_dwordx4 v14, s[16:19], s78 offen offset:2048 lds\n"
"s_waitcnt lgkmcnt(1)\n"
"v_mfma_f32_32x32x16_bf16 v[32:47], a[100:103], a[4:7], v[32:47]\n"
"ds_read_b64_tr_b16 a[86:87], v181 offset:20992\n"
"ds_read_b64_tr_b16 a[88:89], v182 offset:20480\n"
"ds_read_b64_tr_b16 a[90:91], v182 offset:20992\n"
"v_mfma_f32_32x32x16_bf16 v[48:63], a[104:107], a[4:7], v[48:63]\n"
"buffer_load_dwordx4 v15, s[16:19], s78 offen offset:3072 lds\n"
"v_mfma_f32_32x32x16_bf16 v[64:79], a[108:111], a[4:7], v[64:79]\n"
"s_mov_b32 s78, 458880\n"
"s_add_u32 m0, s85, 131072\n"
"ds_read_b64_tr_b16 a[92:93], v183 offset:20480\n"
"ds_read_b64_tr_b16 a[94:95], v183 offset:20992\n"
"ds_read_b64_tr_b16 a[96:97], v180 offset:22528\n"
"v_mfma_f32_32x32x16_bf16 v[16:31], a[80:83], a[8:11], v[16:31]\n"
"buffer_load_dwordx4 v12, s[16:19], s78 offen offset:0 lds\n"
"s_waitcnt lgkmcnt(3)\n"
"v_mfma_f32_32x32x16_bf16 v[32:47], a[84:87], a[8:11], v[32:47]\n"
"ds_read_b64_tr_b16 a[98:99], v180 offset:23040\n"
"buffer_load_dwordx4 v13, s[16:19], s78 offen offset:1024 lds\n"
"v_mfma_f32_32x32x16_bf16 v[48:63], a[88:91], a[8:11], v[48:63]\n"
"ds_read_b64_tr_b16 a[100:101], v181 offset:22528\n"
"ds_read_b64_tr_b16 a[102:103], v181 offset:23040\n"
"ds_read_b64_tr_b16 a[104:105], v182 offset:22528\n"
"s_waitcnt lgkmcnt(5)\n"
"v_mfma_f32_32x32x16_bf16 v[64:79], a[92:95], a[8:11], v[64:79]\n"
"ds_read_b64_tr_b16 a[106:107], v182 offset:23040\n"
"buffer_load_dwordx4 v14, s[16:19], s78 offen offset:2048 lds\n"
"s_waitcnt lgkmcnt(4)\n"
"v_mfma_f32_32x32x16_bf16 v[16:31], a[96:99], a[12:15], v[16:31]\n"
"ds_read_b64_tr_b16 a[108:109], v183 offset:22528\n"
"ds_read_b64_tr_b16 a[110:111], v183 offset:23040\n"
"buffer_load_dword v226, v224, s[24:27], s50 offen sc1\n"
"s_waitcnt lgkmcnt(4)\n"
"v_mfma_f32_32x32x16_bf16 v[32:47], a[100:103], a[12:15], v[32:47]\n"
"ds_read_b64_tr_b16 a[80:81], v180 offset:32768\n"
"ds_read_b64_tr_b16 a[82:83], v180 offset:33280\n"
"ds_read_b64_tr_b16 a[84:85], v181 offset:32768\n"
"s_waitcnt lgkmcnt(5)\n"
"v_mfma_f32_32x32x16_bf16 v[48:63], a[104:107], a[12:15], v[48:63]\n"
"ds_read_b64_tr_b16 a[86:87], v181 offset:33280\n"
"buffer_load_dwordx4 v15, s[16:19], s78 offen offset:3072 lds\n"
"s_waitcnt lgkmcnt(4)\n"
"v_mfma_f32_32x32x16_bf16 v[64:79], a[108:111], a[12:15], v[64:79]\n"
"ds_read_b64_tr_b16 a[88:89], v182 offset:32768\n"
"ds_read_b64_tr_b16 a[90:91], v182 offset:33280\n"
"s_mov_b32 s78, 688256\n"
"s_add_u32 m0, s85, 65536\n"
"ds_read_b64_tr_b16 a[92:93], v183 offset:32768\n"
"s_barrier\n"
"s_waitcnt lgkmcnt(5)\n"
"v_mfma_f32_32x32x16_bf16 v[16:31], a[80:83], a[16:19], v[16:31]\n"
"ds_read_b64_tr_b16 a[94:95], v183 offset:33280\n"
"buffer_load_dwordx4 v12, s[16:19], s78 offen offset:0 lds\n"
"s_waitcnt lgkmcnt(4)\n"
"v_mfma_f32_32x32x16_bf16 v[32:47], a[84:87], a[16:19], v[32:47]\n"
"ds_read_b64_tr_b16 a[96:97], v180 offset:34816\n"
"ds_read_b64_tr_b16 a[98:99], v180 offset:35328\n"
"ds_read_b64_tr_b16 a[100:101], v181 offset:34816\n"
"s_waitcnt lgkmcnt(5)\n"
"v_mfma_f32_32x32x16_bf16 v[48:63], a[88:91], a[16:19], v[48:63]\n"
"ds_read_b64_tr_b16 a[102:103], v181 offset:35328\n"
"buffer_load_dwordx4 v13, s[16:19], s78 offen offset:1024 lds\n"
"s_waitcnt lgkmcnt(4)\n"
"v_mfma_f32_32x32x16_bf16 v[64:79], a[92:95], a[16:19], v[64:79]\n"
"ds_read_b64_tr_b16 a[104:105], v182 offset:34816\n"
"ds_read_b64_tr_b16 a[106:107], v182 offset:35328\n"
"ds_read_b64_tr_b16 a[108:109], v183 offset:34816\n"
"s_waitcnt vmcnt(15)\n"
"s_waitcnt lgkmcnt(5)\n"
"v_mfma_f32_32x32x16_bf16 v[16:31], a[96:99], a[20:23], v[16:31]\n"
"ds_read_b64_tr_b16 a[110:111], v183 offset:35328\n"
"buffer_load_dwordx4 v14, s[16:19], s78 offen offset:2048 lds\n"
"s_waitcnt lgkmcnt(4)\n"
"v_mfma_f32_32x32x16_bf16 v[32:47], a[100:103], a[20:23], v[32:47]\n"
"ds_read_b64_tr_b16 a[80:81], v180 offset:36864\n"
"ds_read_b64_tr_b16 a[82:83], v180 offset:37376\n"
"ds_read_b64_tr_b16 a[84:85], v181 offset:36864\n"
"s_waitcnt lgkmcnt(5)\n"
"v_mfma_f32_32x32x16_bf16 v[48:63], a[104:107], a[20:23], v[48:63]\n"
"ds_read_b64_tr_b16 a[86:87], v181 offset:37376\n"
"buffer_load_dwordx4 v15, s[16:19], s78 offen offset:3072 lds\n"
"s_waitcnt lgkmcnt(4)\n"
"v_mfma_f32_32x32x16_bf16 v[64:79], a[108:111], a[20:23], v[64:79]\n"
"ds_read_b64_tr_b16 a[88:89], v182 offset:36864\n"
"ds_read_b64_tr_b16 a[90:91], v182 offset:37376\n"
"s_mov_b32 s78, 917632\n"
"s_add_u32 m0, s85, 81920\n"
"ds_read_b64_tr_b16 a[92:93], v183 offset:36864\n"
"s_waitcnt lgkmcnt(5)\n"
"v_mfma_f32_32x32x16_bf16 v[16:31], a[80:83], a[24:27], v[16:31]\n"
"ds_read_b64_tr_b16 a[94:95], v183 offset:37376\n"
"buffer_load_dwordx4 v12, s[16:19], s78 offen offset:0 lds\n"
"s_waitcnt lgkmcnt(4)\n"
"v_mfma_f32_32x32x16_bf16 v[32:47], a[84:87], a[24:27], v[32:47]\n"
"ds_read_b64_tr_b16 a[96:97], v180 offset:38912\n"
"ds_read_b64_tr_b16 a[98:99], v180 offset:39424\n"
"ds_read_b64_tr_b16 a[100:101], v181 offset:38912\n"
"s_waitcnt lgkmcnt(5)\n"
"v_mfma_f32_32x32x16_bf16 v[48:63], a[88:91], a[24:27], v[48:63]\n"
"ds_read_b64_tr_b16 a[102:103], v181 offset:39424\n"
"buffer_load_dwordx4 v13, s[16:19], s78 offen offset:1024 lds\n"
"s_waitcnt lgkmcnt(4)\n"
"v_mfma_f32_32x32x16_bf16 v[64:79], a[92:95], a[24:27], v[64:79]\n"
"ds_read_b64_tr_b16 a[104:105], v182 offset:38912\n"
"ds_read_b64_tr_b16 a[106:107], v182 offset:39424\n"
"ds_read_b64_tr_b16 a[108:109], v183 offset:38912\n"
"s_waitcnt lgkmcnt(5)\n"
"v_mfma_f32_32x32x16_bf16 v[16:31], a[96:99], a[28:31], v[16:31]\n"
"ds_read_b64_tr_b16 a[110:111], v183 offset:39424\n"
"buffer_load_dwordx4 v14, s[16:19], s78 offen offset:2048 lds\n"
"s_waitcnt lgkmcnt(4)\n"
"v_mfma_f32_32x32x16_bf16 v[32:47], a[100:103], a[28:31], v[32:47]\n"
"ds_read_b64_tr_b16 a[80:81], v180 offset:49152\n"
"ds_read_b64_tr_b16 a[82:83], v180 offset:49664\n"
"ds_read_b64_tr_b16 a[84:85], v181 offset:49152\n"
"s_waitcnt lgkmcnt(5)\n"
"v_mfma_f32_32x32x16_bf16 v[48:63], a[104:107], a[28:31], v[48:63]\n"
"ds_read_b64_tr_b16 a[86:87], v181 offset:49664\n"
"buffer_load_dwordx4 v15, s[16:19], s78 offen offset:3072 lds\n"
"s_waitcnt lgkmcnt(4)\n"
"v_mfma_f32_32x32x16_bf16 v[64:79], a[108:111], a[28:31], v[64:79]\n"
"ds_read_b64_tr_b16 a[88:89], v182 offset:49152\n"
"ds_read_b64_tr_b16 a[90:91], v182 offset:49664\n"
"ds_read_b64_tr_b16 a[92:93], v183 offset:49152\n"
"s_barrier\n"
"s_waitcnt lgkmcnt(5)\n"
"v_mfma_f32_32x32x16_bf16 v[16:31], a[80:83], a[32:35], v[16:31]\n"
"ds_read_b64_tr_b16 a[94:95], v183 offset:49664\n"
"s_mov_b32 s78, 1147008\n"
"s_add_u32 m0, s85, 98304\n"
"ds_read_b64_tr_b16 a[96:97], v180 offset:51200\n"
"ds_read_b64_tr_b16 a[98:99], v180 offset:51712\n"
"s_waitcnt lgkmcnt(6)\n"
"v_mfma_f32_32x32x16_bf16 v[32:47], a[84:87], a[32:35], v[32:47]\n"
"buffer_load_dwordx4 v12, s[16:19], s78 offen offset:0 lds\n"
"s_waitcnt lgkmcnt(4)\n"
"v_mfma_f32_32x32x16_bf16 v[48:63], a[88:91], a[32:35], v[48:63]\n"
"ds_read_b64_tr_b16 a[100:101], v181 offset:51200\n"
"ds_read_b64_tr_b16 a[102:103], v181 offset:51712\n"
"ds_read_b64_tr_b16 a[104:105], v182 offset:51200\n"
"s_waitcnt lgkmcnt(3)\n"
"v_mfma_f32_32x32x16_bf16 v[64:79], a[92:95], a[32:35], v[64:79]\n"
"ds_read_b64_tr_b16 a[106:107], v182 offset:51712\n"
"buffer_load_dwordx4 v13, s[16:19], s78 offen offset:1024 lds\n"
"v_mfma_f32_32x32x16_bf16 v[16:31], a[96:99], a[36:39], v[16:31]\n"
"ds_read_b64_tr_b16 a[108:109], v183 offset:51200\n"
"ds_read_b64_tr_b16 a[110:111], v183 offset:51712\n"
"ds_read_b64_tr_b16 a[80:81], v180 offset:53248\n"
"s_waitcnt lgkmcnt(5)\n"
"v_mfma_f32_32x32x16_bf16 v[32:47], a[100:103], a[36:39], v[32:47]\n"
"ds_read_b64_tr_b16 a[82:83], v180 offset:53760\n"
"s_waitcnt vmcnt(11)\n"
"v_cmp_ne_u32 vcc, v225, v226\n"
"s_nop 0\n"
"s_cbranch_vccnz L_fs_38_%=\n"
"L_fd_39_%=:\n"
"v_and_b32 v202, 0x1f0, v227\n"
"s_lshl_b32 s75, s61, 10\n"
"s_add_u32 s75, s75, 4947968\n"
"buffer_load_dwordx4 a[224:227], v202, s[24:27], s75 offen offset:0 sc1\n"
"buffer_load_dwordx4 a[228:231], v202, s[24:27], s75 offen offset:512 sc1\n"
"s_waitcnt lgkmcnt(4)\n"
"v_mfma_f32_32x32x16_bf16 v[48:63], a[104:107], a[36:39], v[48:63]\n"
"ds_read_b64_tr_b16 a[84:85], v181 offset:53248\n"
"ds_read_b64_tr_b16 a[86:87], v181 offset:53760\n"
"ds_read_b64_tr_b16 a[88:89], v182 offset:53248\n"
"s_waitcnt lgkmcnt(5)\n"
"v_mfma_f32_32x32x16_bf16 v[64:79], a[108:111], a[36:39], v[64:79]\n"
"ds_read_b64_tr_b16 a[90:91], v182 offset:53760\n"
"buffer_load_dwordx4 v14, s[16:19], s78 offen offset:2048 lds\n"
"s_waitcnt lgkmcnt(4)\n"
"v_mfma_f32_32x32x16_bf16 v[16:31], a[80:83], a[40:43], v[16:31]\n"
"ds_read_b64_tr_b16 a[92:93], v183 offset:53248\n"
"ds_read_b64_tr_b16 a[94:95], v183 offset:53760\n"
"ds_read_b64_tr_b16 a[96:97], v180 offset:55296\n"
"s_waitcnt lgkmcnt(5)\n"
"v_mfma_f32_32x32x16_bf16 v[32:47], a[84:87], a[40:43], v[32:47]\n"
"ds_read_b64_tr_b16 a[98:99], v180 offset:55808\n"
"buffer_load_dwordx4 v15, s[16:19], s78 offen offset:3072 lds\n"
"s_waitcnt lgkmcnt(4)\n"
"v_mfma_f32_32x32x16_bf16 v[48:63], a[88:91], a[40:43], v[48:63]\n"
"ds_read_b64_tr_b16 a[100:101], v181 offset:55296\n"
"ds_read_b64_tr_b16 a[102:103], v181 offset:55808\n"
"ds_read_b64_tr_b16 a[104:105], v182 offset:55296\n"
"s_waitcnt lgkmcnt(5)\n"
"v_mfma_f32_32x32x16_bf16 v[64:79], a[92:95], a[40:43], v[64:79]\n"
"ds_read_b64_tr_b16 a[106:107], v182 offset:55808\n"
"ds_read_b64_tr_b16 a[108:109], v183 offset:55296\n"
"ds_read_b64_tr_b16 a[110:111], v183 offset:55808\n"
"s_waitcnt lgkmcnt(6)\n"
"v_mfma_f32_32x32x16_bf16 v[16:31], a[96:99], a[44:47], v[16:31]\n"
"ds_read_b64_tr_b16 a[80:81], v176 offset:0\n"
"ds_read_b64_tr_b16 a[82:83], v176 offset:512\n"
"ds_read_b64_tr_b16 a[84:85], v177 offset:0\n"
"s_waitcnt lgkmcnt(7)\n"
"v_mfma_f32_32x32x16_bf16 v[32:47], a[100:103], a[44:47], v[32:47]\n"
"ds_read_b64_tr_b16 a[86:87], v177 offset:512\n"
"ds_read_b64_tr_b16 a[88:89], v178 offset:0\n"
"ds_read_b64_tr_b16 a[90:91], v178 offset:512\n"
"s_waitcnt lgkmcnt(6)\n"
"v_mfma_f32_32x32x16_bf16 v[48:63], a[104:107], a[44:47], v[48:63]\n"
"ds_read_b64_tr_b16 a[92:93], v179 offset:0\n"
"ds_read_b64_tr_b16 a[94:95], v179 offset:512\n"
"ds_read_b64_tr_b16 a[96:97], v176 offset:2048\n"
"v_mfma_f32_32x32x16_bf16 v[64:79], a[108:111], a[44:47], v[64:79]\n"
"ds_read_b64_tr_b16 a[98:99], v176 offset:2560\n"
"ds_read_b64_tr_b16 a[100:101], v177 offset:2048\n"
"ds_read_b64_tr_b16 a[102:103], v177 offset:2560\n"
"s_barrier\n"
"s_waitcnt lgkmcnt(6)\n"
"v_mfma_f32_32x32x16_bf16 v[16:31], a[80:83], a[48:51], v[16:31]\n"
"s_mov_b32 s78, 1376384\n"
"s_add_u32 m0, s85, 114688\n"
"ds_read_b64_tr_b16 a[104:105], v178 offset:2048\n"
"ds_read_b64_tr_b16 a[106:107], v178 offset:2560\n"
"ds_read_b64_tr_b16 a[108:109], v179 offset:2048\n"
"v_mfma_f32_32x32x16_bf16 v[32:47], a[84:87], a[48:51], v[32:47]\n"
"buffer_load_dwordx4 v12, s[16:19], s78 offen offset:0 lds\n"
"v_mfma_f32_32x32x16_bf16 v[48:63], a[88:91], a[48:51], v[48:63]\n"
"buffer_load_dwordx4 v13, s[16:19], s78 offen offset:1024 lds\n"
"s_waitcnt lgkmcnt(1)\n"
"v_mfma_f32_32x32x16_bf16 v[64:79], a[92:95], a[48:51], v[64:79]\n"
"buffer_load_dwordx4 v14, s[16:19], s78 offen offset:2048 lds\n"
"v_mfma_f32_32x32x16_bf16 v[16:31], a[96:99], a[52:55], v[16:31]\n"
"ds_read_b64_tr_b16 a[110:111], v179 offset:2560\n"
"buffer_load_dwordx4 v15, s[16:19], s78 offen offset:3072 lds\n"
"v_mfma_f32_32x32x16_bf16 v[32:47], a[100:103], a[52:55], v[32:47]\n"
"ds_read_b64_tr_b16 a[80:81], v176 offset:4096\n"
"ds_read_b64_tr_b16 a[82:83], v176 offset:4608\n"
"ds_read_b64_tr_b16 a[84:85], v177 offset:4096\n"
"v_mfma_f32_32x32x16_bf16 v[48:63], a[104:107], a[52:55], v[48:63]\n"
"ds_read_b64_tr_b16 a[86:87], v177 offset:4608\n"
"ds_read_b64_tr_b16 a[88:89], v178 offset:4096\n"
"ds_read_b64_tr_b16 a[90:91], v178 offset:4608\n"
"s_waitcnt lgkmcnt(6)\n"
"v_mfma_f32_32x32x16_bf16 v[64:79], a[108:111], a[52:55], v[64:79]\n"
"ds_read_b64_tr_b16 a[92:93], v179 offset:4096\n"
"ds_read_b64_tr_b16 a[94:95], v179 offset:4608\n"
"ds_read_b64_tr_b16 a[96:97], v176 offset:6144\n"
"s_waitcnt lgkmcnt(7)\n"
"v_mfma_f32_32x32x16_bf16 v[16:31], a[80:83], a[56:59], v[16:31]\n"
"ds_read_b64_tr_b16 a[98:99], v176 offset:6656\n"
"ds_read_b64_tr_b16 a[100:101], v177 offset:6144\n"
"ds_read_b64_tr_b16 a[102:103], v177 offset:6656\n"
"s_waitcnt lgkmcnt(6)\n"
"v_mfma_f32_32x32x16_bf16 v[32:47], a[84:87], a[56:59], v[32:47]\n"
"ds_read_b64_tr_b16 a[104:105], v178 offset:6144\n"
"ds_read_b64_tr_b16 a[106:107], v178 offset:6656\n"
"ds_read_b64_tr_b16 a[108:109], v179 offset:6144\n"
"v_mfma_f32_32x32x16_bf16 v[48:63], a[88:91], a[56:59], v[48:63]\n"
"ds_read_b64_tr_b16 a[110:111], v179 offset:6656\n"
"ds_read_b64_tr_b16 a[80:81], v176 offset:16384\n"
"ds_read_b64_tr_b16 a[82:83], v176 offset:16896\n"
"s_waitcnt lgkmcnt(6)\n"
"v_mfma_f32_32x32x16_bf16 v[64:79], a[92:95], a[56:59], v[64:79]\n"
"ds_read_b64_tr_b16 a[84:85], v176 offset:18432\n"
"ds_read_b64_tr_b16 a[86:87], v176 offset:18944\n"
"ds_read_b64_tr_b16 a[88:89], v176 offset:20480\n"
"v_mfma_f32_32x32x16_bf16 v[16:31], a[96:99], a[60:63], v[16:31]\n"
"ds_read_b64_tr_b16 a[90:91], v176 offset:20992\n"
"ds_read_b64_tr_b16 a[92:93], v176 offset:22528\n"
"ds_read_b64_tr_b16 a[94:95], v176 offset:23040\n"
"v_mfma_f32_32x32x16_bf16 v[32:47], a[100:103], a[60:63], v[32:47]\n"
"ds_read_b64_tr_b16 a[96:97], v177 offset:16384\n"
"ds_read_b64_tr_b16 a[98:99], v177 offset:16896\n"
"s_waitcnt lgkmcnt(6)\n"
"v_mfma_f32_32x32x16_bf16 v[48:63], a[104:107], a[60:63], v[48:63]\n"
"ds_read_b64_tr_b16 a[100:101], v177 offset:18432\n"
"ds_read_b64_tr_b16 a[102:103], v177 offset:18944\n"
"v_mfma_f32_32x32x16_bf16 v[64:79], a[108:111], a[60:63], v[64:79]\n"
"ds_read_b64_tr_b16 a[104:105], v177 offset:20480\n"
"ds_read_b64_tr_b16 a[106:107], v177 offset:20992\n"
"s_barrier\n"
"s_waitcnt vmcnt(7)\n"
"v_mfma_f32_32x32x16_bf16 v[16:31], a[224:227], a[234:237], v[16:31]\n"
"s_add_u32 s78, s35, 0\n"
"s_mov_b32 m0, s85\n"
"v_mfma_f32_32x32x16_bf16 v[32:47], a[224:227], a[240:243], v[32:47]\n"
"buffer_load_dwordx4 v194, s[20:23], s78 offen offset:0 lds\n"
"v_mfma_f32_32x32x16_bf16 v[48:63], a[224:227], a[232:235], v[48:63]\n"
"buffer_load_dwordx4 v195, s[20:23], s78 offen offset:1024 lds\n"
"v_mfma_f32_32x32x16_bf16 v[64:79], a[224:227], a[238:241], v[64:79]\n"
"buffer_load_dwordx4 v196, s[20:23], s78 offen offset:2048 lds\n"
"v_mfma_f32_32x32x16_bf16 v[16:31], a[80:83], a[64:67], v[16:31]\n"
"buffer_load_dwordx4 v197, s[20:23], s78 offen offset:3072 lds\n"
"v_mfma_f32_32x32x16_bf16 v[16:31], a[84:87], a[68:71], v[16:31]\n"
"ds_read_b64_tr_b16 a[108:109], v177 offset:22528\n"
"ds_read_b64_tr_b16 a[110:111], v177 offset:23040\n"
"ds_read_b64_tr_b16 a[80:81], v178 offset:16384\n"
"s_waitcnt lgkmcnt(3)\n"
"v_mfma_f32_32x32x16_bf16 v[16:31], a[88:91], a[72:75], v[16:31]\n"
"ds_read_b64_tr_b16 a[82:83], v178 offset:16896\n"
"ds_read_b64_tr_b16 a[84:85], v178 offset:18432\n"
"ds_read_b64_tr_b16 a[86:87], v178 offset:18944\n"
"v_mfma_f32_32x32x16_bf16 v[16:31], a[92:95], a[76:79], v[16:31]\n"
"ds_read_b64_tr_b16 a[88:89], v178 offset:20480\n"
"ds_read_b64_tr_b16 a[90:91], v178 offset:20992\n"
"v_mfma_f32_32x32x16_bf16 v[32:47], a[96:99], a[64:67], v[32:47]\n"
"buffer_load_dword v226, off, s[24:27], s36 sc1\n"
"ds_read_b64_tr_b16 a[92:93], v178 offset:22528\n"
"ds_read_b64_tr_b16 a[94:95], v178 offset:23040\n"
"v_mfma_f32_32x32x16_bf16 v[32:47], a[100:103], a[68:71], v[32:47]\n"
"s_nop 4\n"
"v_cmp_u_f32 vcc, v16, v16\n"
"ds_write_b32 v241, v16\n"
"s_and_b64 vcc, vcc, s[56:57]\n"
"s_cselect_b32 s49, 2130706432, s40\n"
"v_mfma_f32_32x32x16_bf16 v[32:47], a[104:107], a[72:75], v[32:47]\n"
"v_mul_f32 v16, v16, v24\n"
"v_mul_f32 v17, v17, v25\n"
"v_cvt_pk_bf16_f32 v16, v16, v17\n"
"v_mul_f32 v18, v18, v26\n"
"v_mul_f32 v19, v19, v27\n"
"s_waitcnt lgkmcnt(1)\n"
"v_mfma_f32_32x32x16_bf16 v[32:47], a[108:111], a[76:79], v[32:47]\n"
"ds_read_b64_tr_b16 a[96:97], v179 offset:16384\n"
"ds_read_b64_tr_b16 a[98:99], v179 offset:16896\n"
"v_cvt_pk_bf16_f32 v17, v18, v19\n"
"v_mul_f32 v20, v20, v28\n"
"v_mul_f32 v21, v21, v29\n"
"v_cvt_pk_bf16_f32 v18, v20, v21\n"
"v_mfma_f32_32x32x16_bf16 v[48:63], a[80:83], a[64:67], v[48:63]\n"
"ds_read_b64_tr_b16 a[100:101], v179 offset:18432\n"
"ds_read_b64_tr_b16 a[102:103], v179 offset:18944\n"
"v_mul_f32 v22, v22, v30\n"
"v_mul_f32 v23, v23, v31\n"
"v_cvt_pk_bf16_f32 v19, v22, v23\n"
"buffer_store_dwordx4 v[16:19], v227, s[24:27], s49 offen offset:0 sc1\n"
"v_mfma_f32_32x32x16_bf16 v[48:63], a[84:87], a[68:71], v[48:63]\n"
"ds_read_b64_tr_b16 a[104:105], v179 offset:20480\n"
"ds_read_b64_tr_b16 a[106:107], v179 offset:20992\n"
"v_mul_f32 v32, v32, v40\n"
"v_mul_f32 v33, v33, v41\n"
"v_cvt_pk_bf16_f32 v32, v32, v33\n"
"v_mul_f32 v34, v34, v42\n"
"v_mfma_f32_32x32x16_bf16 v[48:63], a[88:91], a[72:75], v[48:63]\n"
"ds_read_b64_tr_b16 a[108:109], v179 offset:22528\n"
"ds_read_b64_tr_b16 a[110:111], v179 offset:23040\n"
"v_mul_f32 v35, v35, v43\n"
"v_cvt_pk_bf16_f32 v33, v34, v35\n"
"v_mul_f32 v36, v36, v44\n"
"v_mul_f32 v37, v37, v45\n"
"v_mfma_f32_32x32x16_bf16 v[48:63], a[92:95], a[76:79], v[48:63]\n"
"ds_read_b64_tr_b16 a[80:81], v176 offset:32768\n"
"ds_read_b64_tr_b16 a[82:83], v176 offset:33280\n"
"v_cvt_pk_bf16_f32 v34, v36, v37\n"
"v_mul_f32 v38, v38, v46\n"
"v_mul_f32 v39, v39, v47\n"
"s_waitcnt lgkmcnt(4)\n"
"v_mfma_f32_32x32x16_bf16 v[64:79], a[96:99], a[64:67], v[64:79]\n"
"ds_read_b64_tr_b16 a[84:85], v177 offset:32768\n"
"ds_read_b64_tr_b16 a[86:87], v177 offset:33280\n"
"v_cvt_pk_bf16_f32 v35, v38, v39\n"
"buffer_store_dwordx4 v[32:35], v227, s[24:27], s49 offen offset:1024 sc1\n"
"v_mfma_f32_32x32x16_bf16 v[64:79], a[100:103], a[68:71], v[64:79]\n"
"ds_read_b64_tr_b16 a[88:89], v178 offset:32768\n"
"ds_read_b64_tr_b16 a[90:91], v178 offset:33280\n"
"v_mul_f32 v48, v48, v56\n"
"v_mul_f32 v49, v49, v57\n"
"v_cvt_pk_bf16_f32 v48, v48, v49\n"
"v_mul_f32 v50, v50, v58\n"
"v_mfma_f32_32x32x16_bf16 v[64:79], a[104:107], a[72:75], v[64:79]\n"
"ds_read_b64_tr_b16 a[92:93], v179 offset:32768\n"
"ds_read_b64_tr_b16 a[94:95], v179 offset:33280\n"
"v_mul_f32 v51, v51, v59\n"
"v_cvt_pk_bf16_f32 v49, v50, v51\n"
"v_mul_f32 v52, v52, v60\n"
"s_waitcnt lgkmcnt(4)\n"
"v_mfma_f32_32x32x16_bf16 v[64:79], a[108:111], a[76:79], v[64:79]\n"
"ds_read_b64_tr_b16 a[96:97], v176 offset:34816\n"
"ds_read_b64_tr_b16 a[98:99], v176 offset:35328\n"
"v_mul_f32 v53, v53, v61\n"
"v_cvt_pk_bf16_f32 v50, v52, v53\n"
"v_mul_f32 v54, v54, v62\n"
"v_mul_f32 v55, v55, v63\n"
"s_barrier\n"
"s_waitcnt vmcnt(13)\n"
"v_mfma_f32_32x32x16_bf16 v[80:95], a[228:231], a[234:237], 0\n"
"v_cvt_pk_bf16_f32 v51, v54, v55\n"
"buffer_store_dwordx4 v[48:51], v227, s[24:27], s49 offen offset:2048 sc1\n"
"ds_read_b64_tr_b16 a[100:101], v177 offset:34816\n"
"ds_read_b64_tr_b16 a[102:103], v177 offset:35328\n"
"v_mfma_f32_32x32x16_bf16 v[96:111], a[228:231], a[240:243], 0\n"
"v_mul_f32 v64, v64, v72\n"
"v_mul_f32 v65, v65, v73\n"
"v_cvt_pk_bf16_f32 v64, v64, v65\n"
"v_mul_f32 v66, v66, v74\n"
"v_mul_f32 v67, v67, v75\n"
"v_cvt_pk_bf16_f32 v65, v66, v67\n"
"v_mfma_f32_32x32x16_bf16 v[112:127], a[228:231], a[232:235], 0\n"
"v_mul_f32 v68, v68, v76\n"
"v_mul_f32 v69, v69, v77\n"
"v_cvt_pk_bf16_f32 v66, v68, v69\n"
"v_mul_f32 v70, v70, v78\n"
"v_mul_f32 v71, v71, v79\n"
"v_cvt_pk_bf16_f32 v67, v70, v71\n"
"buffer_store_dwordx4 v[64:67], v227, s[24:27], s49 offen offset:3072 sc1\n"
"v_mfma_f32_32x32x16_bf16 v[128:143], a[228:231], a[238:241], 0\n"
"s_add_u32 s78, s35, 229376\n"
"s_add_u32 m0, s85, 16384\n"
"ds_read_b64_tr_b16 a[104:105], v178 offset:34816\n"
"ds_read_b64_tr_b16 a[106:107], v178 offset:35328\n"
"v_mfma_f32_32x32x16_bf16 v[80:95], a[80:83], v[144:147], v[80:95]\n"
"ds_read_b64_tr_b16 a[108:109], v179 offset:34816\n"
"ds_read_b64_tr_b16 a[110:111], v179 offset:35328\n"
"ds_read_b128 v[228:231], v240\n"
"ds_read_b32 v220, v221\n"
"v_mfma_f32_32x32x16_bf16 v[96:111], a[84:87], v[144:147], v[96:111]\n"
"s_waitcnt lgkmcnt(4)\n"
"v_mfma_f32_32x32x16_bf16 v[112:127], a[88:91], v[144:147], v[112:127]\n"
"buffer_load_dwordx4 v194, s[20:23], s78 offen offset:0 lds\n"
"v_mfma_f32_32x32x16_bf16 v[128:143], a[92:95], v[144:147], v[128:143]\n"
"buffer_load_dwordx4 v195, s[20:23], s78 offen offset:1024 lds\n"
"v_mfma_f32_32x32x16_bf16 v[80:95], a[96:99], v[148:151], v[80:95]\n"
"ds_read_b64_tr_b16 a[80:81], v176 offset:36864\n"
"ds_read_b64_tr_b16 a[82:83], v176 offset:37376\n"
"ds_read_b64_tr_b16 a[84:85], v177 offset:36864\n"
"v_mfma_f32_32x32x16_bf16 v[96:111], a[100:103], v[148:151], v[96:111]\n"
"buffer_load_dwordx4 v196, s[20:23], s78 offen offset:2048 lds\n"
"v_mfma_f32_32x32x16_bf16 v[112:127], a[104:107], v[148:151], v[112:127]\n"
"ds_read_b64_tr_b16 a[86:87], v177 offset:37376\n"
"ds_read_b64_tr_b16 a[88:89], v178 offset:36864\n"
"ds_read_b64_tr_b16 a[90:91], v178 offset:37376\n"
"s_waitcnt lgkmcnt(4)\n"
"v_mfma_f32_32x32x16_bf16 v[128:143], a[108:111], v[148:151], v[128:143]\n"
"buffer_load_dwordx4 v197, s[20:23], s78 offen offset:3072 lds\n"
"v_mfma_f32_32x32x16_bf16 v[80:95], a[80:83], v[152:155], v[80:95]\n"
"ds_read_b64_tr_b16 a[92:93], v179 offset:36864\n"
"ds_read_b64_tr_b16 a[94:95], v179 offset:37376\n"
"ds_read_b64_tr_b16 a[96:97], v176 offset:38912\n"
"s_waitcnt lgkmcnt(3)\n"
"v_mfma_f32_32x32x16_bf16 v[96:111], a[84:87], v[152:155], v[96:111]\n"
"ds_read_b64_tr_b16 a[98:99], v176 offset:39424\n"
"v_cmp_u_f32 vcc, v228, v229\n"
"v_cmp_u_f32 s[66:67], v230, v231\n"
"s_nop 0\n"
"s_or_b64 vcc, vcc, s[66:67]\n"
"s_and_b64 vcc, vcc, s[56:57]\n"
"s_cbranch_vccnz L_restart_37_%=\n"
"v_mfma_f32_32x32x16_bf16 v[112:127], a[88:91], v[152:155], v[112:127]\n"
"ds_read_b64_tr_b16 a[100:101], v177 offset:38912\n"
"ds_read_b64_tr_b16 a[102:103], v177 offset:39424\n"
"ds_read_b64_tr_b16 a[104:105], v178 offset:38912\n"
"s_waitcnt lgkmcnt(5)\n"
"v_mfma_f32_32x32x16_bf16 v[128:143], a[92:95], v[152:155], v[128:143]\n"
"ds_read_b64_tr_b16 a[106:107], v178 offset:39424\n"
"ds_read_b64_tr_b16 a[108:109], v179 offset:38912\n"
"ds_read_b64_tr_b16 a[110:111], v179 offset:39424\n"
"s_waitcnt lgkmcnt(6)\n"
"v_mfma_f32_32x32x16_bf16 v[80:95], a[96:99], v[156:159], v[80:95]\n"
"ds_read_b64_tr_b16 a[80:81], v176 offset:49152\n"
"ds_read_b64_tr_b16 a[82:83], v176 offset:49664\n"
"ds_read_b64_tr_b16 a[84:85], v177 offset:49152\n"
"s_waitcnt lgkmcnt(7)\n"
"v_mfma_f32_32x32x16_bf16 v[96:111], a[100:103], v[156:159], v[96:111]\n"
"ds_read_b64_tr_b16 a[86:87], v177 offset:49664\n"
"ds_read_b64_tr_b16 a[88:89], v178 offset:49152\n"
"ds_read_b64_tr_b16 a[90:91], v178 offset:49664\n"
"s_waitcnt lgkmcnt(6)\n"
"v_mfma_f32_32x32x16_bf16 v[112:127], a[104:107], v[156:159], v[112:127]\n"
"ds_read_b64_tr_b16 a[92:93], v179 offset:49152\n"
"ds_read_b64_tr_b16 a[94:95], v179 offset:49664\n"
"ds_read_b64_tr_b16 a[96:97], v176 offset:51200\n"
"v_mfma_f32_32x32x16_bf16 v[128:143], a[108:111], v[156:159], v[128:143]\n"
"ds_read_b64_tr_b16 a[98:99], v176 offset:51712\n"
"ds_read_b64_tr_b16 a[100:101], v177 offset:51200\n"
"ds_read_b64_tr_b16 a[102:103], v177 offset:51712\n"
"s_barrier\n"
"s_waitcnt lgkmcnt(6)\n"
"v_mfma_f32_32x32x16_bf16 v[80:95], a[80:83], v[160:163], v[80:95]\n"
"s_add_u32 s78, s35, 458752\n"
"s_add_u32 m0, s85, 32768\n"
"ds_read_b64_tr_b16 a[104:105], v178 offset:51200\n"
"ds_read_b64_tr_b16 a[106:107], v178 offset:51712\n"
"ds_read_b64_tr_b16 a[108:109], v179 offset:51200\n"
"s_mul_i32 s75, s81, 114688\n"
"v_mfma_f32_32x32x16_bf16 v[96:111], a[84:87], v[160:163], v[96:111]\n"
"buffer_load_dwordx4 v194, s[20:23], s78 offen offset:0 lds\n"
"v_mfma_f32_32x32x16_bf16 v[112:127], a[88:91], v[160:163], v[112:127]\n"
"buffer_load_dwordx4 v195, s[20:23], s78 offen offset:1024 lds\n"
"s_waitcnt lgkmcnt(1)\n"
"v_mfma_f32_32x32x16_bf16 v[128:143], a[92:95], v[160:163], v[128:143]\n"
"buffer_load_dwordx4 v196, s[20:23], s78 offen offset:2048 lds\n"
"v_mfma_f32_32x32x16_bf16 v[80:95], a[96:99], v[164:167], v[80:95]\n"
"ds_read_b64_tr_b16 a[110:111], v179 offset:51712\n"
"buffer_load_dwordx4 v197, s[20:23], s78 offen offset:3072 lds\n"
"v_mfma_f32_32x32x16_bf16 v[96:111], a[100:103], v[164:167], v[96:111]\n"
"ds_read_b64_tr_b16 a[80:81], v176 offset:53248\n"
"ds_read_b64_tr_b16 a[82:83], v176 offset:53760\n"
"s_add_u32 s78, s35, 286720\n"
"ds_read_b64_tr_b16 a[84:85], v177 offset:53248\n"
"s_lshl_b32 s76, s82, 12\n"
"s_add_u32 s75, s75, s76\n"
"v_mfma_f32_32x32x16_bf16 v[112:127], a[104:107], v[164:167], v[112:127]\n"
"ds_read_b64_tr_b16 a[86:87], v177 offset:53760\n"
"buffer_load_dwordx4 v[16:19], v194, s[20:23], s78 offen offset:0\n"
"ds_read_b64_tr_b16 a[88:89], v178 offset:53248\n"
"s_waitcnt lgkmcnt(5)\n"
"v_mfma_f32_32x32x16_bf16 v[128:143], a[108:111], v[164:167], v[128:143]\n"
"ds_read_b64_tr_b16 a[90:91], v178 offset:53760\n"
"buffer_load_dwordx4 v[20:23], v195, s[20:23], s78 offen offset:1024\n"
"ds_read_b64_tr_b16 a[92:93], v179 offset:53248\n"
"s_waitcnt lgkmcnt(5)\n"
"v_mfma_f32_32x32x16_bf16 v[80:95], a[80:83], v[168:171], v[80:95]\n"
"ds_read_b64_tr_b16 a[94:95], v179 offset:53760\n"
"buffer_load_dwordx4 v[24:27], v196, s[20:23], s78 offen offset:2048\n"
"ds_read_b64_tr_b16 a[96:97], v176 offset:55296\n"
"s_waitcnt lgkmcnt(5)\n"
"v_mfma_f32_32x32x16_bf16 v[96:111], a[84:87], v[168:171], v[96:111]\n"
"ds_read_b64_tr_b16 a[98:99], v176 offset:55808\n"
"ds_read_b64_tr_b16 a[100:101], v177 offset:55296\n"
"ds_read_b64_tr_b16 a[102:103], v177 offset:55808\n"
"s_add_u32 s75, s75, 6291456\n"
"s_add_u32 s41, s75, 0\n"
"s_waitcnt lgkmcnt(6)\n"
"v_mfma_f32_32x32x16_bf16 v[112:127], a[88:91], v[168:171], v[112:127]\n"
"buffer_load_dwordx4 v[28:31], v197, s[20:23], s78 offen offset:3072\n"
"ds_read_b64_tr_b16 a[104:105], v178 offset:55296\n"
"ds_read_b64_tr_b16 a[106:107], v178 offset:55808\n"
"s_waitcnt lgkmcnt(6)\n"
"v_mfma_f32_32x32x16_bf16 v[128:143], a[92:95], v[168:171], v[128:143]\n"
"s_add_u32 s78, s35, 516096\n"
"ds_read_b64_tr_b16 a[108:109], v179 offset:55296\n"
"ds_read_b64_tr_b16 a[110:111], v179 offset:55808\n"
"ds_read_b64_tr_b16 a[80:81], v184 offset:0\n"
"s_add_u32 s42, s75, 8192\n"
"s_waitcnt lgkmcnt(5)\n"
"v_mfma_f32_32x32x16_bf16 v[80:95], a[96:99], v[172:175], v[80:95]\n"
"buffer_load_dwordx4 v[32:35], v194, s[20:23], s78 offen offset:0\n"
"ds_read_b64_tr_b16 a[82:83], v184 offset:512\n"
"ds_read_b64_tr_b16 a[84:85], v185 offset:0\n"
"ds_read_b64_tr_b16 a[86:87], v185 offset:512\n"
"v_mfma_f32_32x32x16_bf16 v[96:111], a[100:103], v[172:175], v[96:111]\n"
"buffer_load_dwordx4 v[36:39], v195, s[20:23], s78 offen offset:1024\n"
"ds_read_b64_tr_b16 a[88:89], v186 offset:0\n"
"ds_read_b64_tr_b16 a[90:91], v186 offset:512\n"
"s_add_u32 s43, s75, 16384\n"
"s_add_u32 s44, s75, 24576\n"
"s_add_u32 s45, s75, 32768\n"
"s_add_u32 s46, s75, 40960\n"
"s_add_u32 s47, s75, 49152\n"
"s_add_u32 s88, s41, 57344\n"
"s_add_u32 s89, s42, 57344\n"
"s_add_u32 s90, s43, 57344\n"
"s_add_u32 s91, s44, 57344\n"
"s_add_u32 s92, s45, 57344\n"
"s_add_u32 s93, s46, 57344\n"
"s_add_u32 s94, s47, 57344\n"
"s_lshl_b32 s51, s80, 11\n"
"s_lshl_b32 s76, s61, 7\n"
"s_add_u32 s51, s51, s76\n"
"s_add_u32 s51, s51, 32768\n"
"s_lshl_b32 s50, s81, 11\n"
"s_lshl_b32 s76, s82, 7\n"
"s_add_u32 s50, s50, s76\n"
"s_add_u32 s50, s50, 32768\n"
"v_and_b32 v224, 7, v1\n"
"v_min_u32 v224, 6, v224\n"
"v_lshlrev_b32 v224, 8, v224\n"
"s_waitcnt vmcnt(18)\n"
"v_cmp_ne_u32 vcc, v225, v226\n"
"s_nop 0\n"
"s_cbranch_vccnz L_fs_40_%=\n"
"L_fd_41_%=:\n"
"s_waitcnt vmcnt(14)\n"
"v_lshlrev_b32 v235, 2, v1\n"
"s_mov_b64 exec, s[52:53]\n"
"buffer_store_dword v225, v235, s[24:27], s51 offen sc1\n"
"s_mov_b64 exec, -1\n"
"s_waitcnt lgkmcnt(6)\n"
"v_mfma_f32_32x32x16_bf16 v[112:127], a[104:107], v[172:175], v[112:127]\n"
"ds_read_b64_tr_b16 a[92:93], v187 offset:0\n"
"ds_read_b64_tr_b16 a[94:95], v187 offset:512\n"
"ds_read_b64_tr_b16 a[96:97], v184 offset:2048\n"
"v_mfma_f32_32x32x16_bf16 v[128:143], a[108:111], v[172:175], v[128:143]\n"
"buffer_load_dwordx4 v[40:43], v196, s[20:23], s78 offen offset:2048\n"
"ds_read_b64_tr_b16 a[98:99], v184 offset:2560\n"
"ds_read_b64_tr_b16 a[100:101], v185 offset:2048\n"
"s_barrier\n"
"s_waitcnt lgkmcnt(5)\n"
"v_mfma_f32_32x32x16_bf16 v[80:95], a[80:83], a[0:3], v[80:95]\n"
"buffer_load_dwordx4 v[44:47], v197, s[20:23], s78 offen offset:3072\n"
"ds_read_b64_tr_b16 a[102:103], v185 offset:2560\n"
"ds_read_b64_tr_b16 a[104:105], v186 offset:2048\n"
"ds_read_b64_tr_b16 a[106:107], v186 offset:2560\n"
"v_mfma_f32_32x32x16_bf16 v[96:111], a[84:87], a[0:3], v[96:111]\n"
"s_add_u32 s78, s35, 688128\n"
"s_add_u32 m0, s85, 49152\n"
"ds_read_b64_tr_b16 a[108:109], v187 offset:2048\n"
"ds_read_b64_tr_b16 a[110:111], v187 offset:2560\n"
"ds_read_b64_tr_b16 a[80:81], v184 offset:4096\n"
"v_mfma_f32_32x32x16_bf16 v[112:127], a[88:91], a[0:3], v[112:127]\n"
"buffer_load_dwordx4 v194, s[20:23], s78 offen offset:0 lds\n"
"s_waitcnt lgkmcnt(3)\n"
"v_mfma_f32_32x32x16_bf16 v[128:143], a[92:95], a[0:3], v[128:143]\n"
"ds_read_b64_tr_b16 a[82:83], v184 offset:4608\n"
"ds_read_b64_tr_b16 a[84:85], v185 offset:4096\n"
"ds_read_b64_tr_b16 a[86:87], v185 offset:4608\n"
"v_mfma_f32_32x32x16_bf16 v[80:95], a[96:99], a[4:7], v[80:95]\n"
"buffer_load_dwordx4 v195, s[20:23], s78 offen offset:1024 lds\n"
"v_mfma_f32_32x32x16_bf16 v[96:111], a[100:103], a[4:7], v[96:111]\n"
"buffer_load_dwordx4 v196, s[20:23], s78 offen offset:2048 lds\n"
"v_mfma_f32_32x32x16_bf16 v[112:127], a[104:107], a[4:7], v[112:127]\n"
"ds_read_b64_tr_b16 a[88:89], v186 offset:4096\n"
"ds_read_b64_tr_b16 a[90:91], v186 offset:4608\n"
"ds_read_b64_tr_b16 a[92:93], v187 offset:4096\n"
"s_waitcnt lgkmcnt(3)\n"
"v_mfma_f32_32x32x16_bf16 v[128:143], a[108:111], a[4:7], v[128:143]\n"
"buffer_load_dwordx4 v197, s[20:23], s78 offen offset:3072 lds\n"
"v_mfma_f32_32x32x16_bf16 v[80:95], a[80:83], a[8:11], v[80:95]\n"
"ds_read_b64_tr_b16 a[94:95], v187 offset:4608\n"
"s_add_u32 s78, s35, 745472\n"
"ds_read_b64_tr_b16 a[96:97], v184 offset:6144\n"
"ds_read_b64_tr_b16 a[98:99], v184 offset:6656\n"
"v_mfma_f32_32x32x16_bf16 v[96:111], a[84:87], a[8:11], v[96:111]\n"
"buffer_load_dwordx4 v[48:51], v194, s[20:23], s78 offen offset:0\n"
"ds_read_b64_tr_b16 a[100:101], v185 offset:6144\n"
"ds_read_b64_tr_b16 a[102:103], v185 offset:6656\n"
"s_waitcnt lgkmcnt(6)\n"
"v_mfma_f32_32x32x16_bf16 v[112:127], a[88:91], a[8:11], v[112:127]\n"
"buffer_load_dwordx4 v[52:55], v195, s[20:23], s78 offen offset:1024\n"
"ds_read_b64_tr_b16 a[104:105], v186 offset:6144\n"
"ds_read_b64_tr_b16 a[106:107], v186 offset:6656\n"
"s_waitcnt lgkmcnt(4)\n"
"v_mfma_f32_32x32x16_bf16 v[128:143], a[92:95], a[8:11], v[128:143]\n"
"ds_read_b64_tr_b16 a[108:109], v187 offset:6144\n"
"ds_read_b64_tr_b16 a[110:111], v187 offset:6656\n"
"ds_read_b64_tr_b16 a[80:81], v180 offset:0\n"
"v_mfma_f32_32x32x16_bf16 v[80:95], a[96:99], a[12:15], v[80:95]\n"
"buffer_load_dwordx4 v[56:59], v196, s[20:23], s78 offen offset:2048\n"
"ds_read_b64_tr_b16 a[82:83], v180 offset:512\n"
"ds_read_b64_tr_b16 a[84:85], v181 offset:0\n"
"s_waitcnt lgkmcnt(5)\n"
"v_mfma_f32_32x32x16_bf16 v[96:111], a[100:103], a[12:15], v[96:111]\n"
"ds_read_b64_tr_b16 a[86:87], v181 offset:512\n"
"ds_read_b64_tr_b16 a[88:89], v182 offset:0\n"
"ds_read_b64_tr_b16 a[90:91], v182 offset:512\n"
"v_mfma_f32_32x32x16_bf16 v[112:127], a[104:107], a[12:15], v[112:127]\n"
"buffer_load_dwordx4 v[60:63], v197, s[20:23], s78 offen offset:3072\n"
"ds_read_b64_tr_b16 a[92:93], v183 offset:0\n"
"ds_read_b64_tr_b16 a[94:95], v183 offset:512\n"
"s_waitcnt lgkmcnt(6)\n"
"v_mfma_f32_32x32x16_bf16 v[128:143], a[108:111], a[12:15], v[128:143]\n"
"s_add_u32 s78, s35, 974848\n"
"ds_read_b64_tr_b16 a[96:97], v180 offset:2048\n"
"ds_read_b64_tr_b16 a[98:99], v180 offset:2560\n"
"ds_read_b64_tr_b16 a[100:101], v181 offset:2048\n"
"s_barrier\n"
"v_mfma_f32_32x32x16_bf16 v[80:95], a[80:83], a[16:19], v[80:95]\n"
"buffer_load_dwordx4 v[64:67], v194, s[20:23], s78 offen offset:0\n"
"ds_read_b64_tr_b16 a[102:103], v181 offset:2560\n"
"ds_read_b64_tr_b16 a[104:105], v182 offset:2048\n"
"s_waitcnt lgkmcnt(5)\n"
"v_mfma_f32_32x32x16_bf16 v[96:111], a[84:87], a[16:19], v[96:111]\n"
"buffer_load_dwordx4 v[68:71], v195, s[20:23], s78 offen offset:1024\n"
"ds_read_b64_tr_b16 a[106:107], v182 offset:2560\n"
"ds_read_b64_tr_b16 a[108:109], v183 offset:2048\n"
"ds_read_b64_tr_b16 a[110:111], v183 offset:2560\n"
"v_mfma_f32_32x32x16_bf16 v[112:127], a[88:91], a[16:19], v[112:127]\n"
"buffer_load_dword v226, v224, s[24:27], s50 offen sc1\n"
"ds_read_b64_tr_b16 a[80:81], v180 offset:4096\n"
"ds_read_b64_tr_b16 a[82:83], v180 offset:4608\n"
"ds_read_b64_tr_b16 a[84:85], v181 offset:4096\n"
"v_mfma_f32_32x32x16_bf16 v[128:143], a[92:95], a[16:19], v[128:143]\n"
"buffer_load_dwordx4 v[72:75], v196, s[20:23], s78 offen offset:2048\n"
"ds_read_b64_tr_b16 a[86:87], v181 offset:4608\n"
"ds_read_b64_tr_b16 a[88:89], v182 offset:4096\n"
"s_waitcnt lgkmcnt(5)\n"
"v_mfma_f32_32x32x16_bf16 v[80:95], a[96:99], a[20:23], v[80:95]\n"
"ds_read_b64_tr_b16 a[90:91], v182 offset:4608\n"
"ds_read_b64_tr_b16 a[92:93], v183 offset:4096\n"
"ds_read_b64_tr_b16 a[94:95], v183 offset:4608\n"
"v_mfma_f32_32x32x16_bf16 v[96:111], a[100:103], a[20:23], v[96:111]\n"
"buffer_load_dwordx4 v[76:79], v197, s[20:23], s78 offen offset:3072\n"
"ds_read_b64_tr_b16 a[96:97], v180 offset:6144\n"
"ds_read_b64_tr_b16 a[98:99], v180 offset:6656\n"
"v_mfma_f32_32x32x16_bf16 v[112:127], a[104:107], a[20:23], v[112:127]\n"
"s_add_u32 s78, s35, 917504\n"
"s_add_u32 m0, s85, 131072\n"
"ds_read_b64_tr_b16 a[100:101], v181 offset:6144\n"
"ds_read_b64_tr_b16 a[102:103], v181 offset:6656\n"
"v_mfma_f32_32x32x16_bf16 v[128:143], a[108:111], a[20:23], v[128:143]\n"
"buffer_load_dwordx4 v194, s[20:23], s78 offen offset:0 lds\n"
"s_waitcnt lgkmcnt(2)\n"
"v_mfma_f32_32x32x16_bf16 v[80:95], a[80:83], a[24:27], v[80:95]\n"
"buffer_load_dwordx4 v195, s[20:23], s78 offen offset:1024 lds\n"
"v_mfma_f32_32x32x16_bf16 v[96:111], a[84:87], a[24:27], v[96:111]\n"
"ds_read_b64_tr_b16 a[104:105], v182 offset:6144\n"
"ds_read_b64_tr_b16 a[106:107], v182 offset:6656\n"
"ds_read_b64_tr_b16 a[108:109], v183 offset:6144\n"
"v_mfma_f32_32x32x16_bf16 v[112:127], a[88:91], a[24:27], v[112:127]\n"
"buffer_load_dwordx4 v196, s[20:23], s78 offen offset:2048 lds\n"
"v_mfma_f32_32x32x16_bf16 v[128:143], a[92:95], a[24:27], v[128:143]\n"
"buffer_load_dwordx4 v197, s[20:23], s78 offen offset:3072 lds\n"
"v_mfma_f32_32x32x16_bf16 v[80:95], a[96:99], a[28:31], v[80:95]\n"
"ds_read_b64_tr_b16 a[110:111], v183 offset:6656\n"
"ds_read_b64_tr_b16 a[80:81], v180 offset:16384\n"
"ds_read_b64_tr_b16 a[82:83], v180 offset:16896\n"
"s_waitcnt lgkmcnt(4)\n"
"v_mfma_f32_32x32x16_bf16 v[96:111], a[100:103], a[28:31], v[96:111]\n"
"ds_read_b64_tr_b16 a[84:85], v181 offset:16384\n"
"ds_read_b64_tr_b16 a[86:87], v181 offset:16896\n"
"ds_read_b64_tr_b16 a[88:89], v182 offset:16384\n"
"v_mfma_f32_32x32x16_bf16 v[112:127], a[104:107], a[28:31], v[112:127]\n"
"ds_read_b64_tr_b16 a[90:91], v182 offset:16896\n"
"ds_read_b64_tr_b16 a[92:93], v183 offset:16384\n"
"ds_read_b64_tr_b16 a[94:95], v183 offset:16896\n"
"s_waitcnt lgkmcnt(6)\n"
"v_mfma_f32_32x32x16_bf16 v[128:143], a[108:111], a[28:31], v[128:143]\n"
"ds_read_b64_tr_b16 a[96:97], v180 offset:18432\n"
"ds_read_b64_tr_b16 a[98:99], v180 offset:18944\n"
"ds_read_b64_tr_b16 a[100:101], v181 offset:18432\n"
"s_barrier\n"
"v_mfma_f32_32x32x16_bf16 v[80:95], a[80:83], a[32:35], v[80:95]\n"
"s_add_u32 s78, s35, 1146880\n"
"s_add_u32 m0, s85, 65536\n"
"ds_read_b64_tr_b16 a[102:103], v181 offset:18944\n"
"ds_read_b64_tr_b16 a[104:105], v182 offset:18432\n"
"ds_read_b64_tr_b16 a[106:107], v182 offset:18944\n"
"s_waitcnt lgkmcnt(6)\n"
"v_mfma_f32_32x32x16_bf16 v[96:111], a[84:87], a[32:35], v[96:111]\n"
"buffer_load_dwordx4 v194, s[20:23], s78 offen offset:0 lds\n"
"v_mfma_f32_32x32x16_bf16 v[112:127], a[88:91], a[32:35], v[112:127]\n"
"buffer_load_dwordx4 v195, s[20:23], s78 offen offset:1024 lds\n"
"v_mfma_f32_32x32x16_bf16 v[128:143], a[92:95], a[32:35], v[128:143]\n"
"buffer_load_dwordx4 v196, s[20:23], s78 offen offset:2048 lds\n"
"s_waitcnt lgkmcnt(0)\n"
"v_mfma_f32_32x32x16_bf16 v[80:95], a[96:99], a[36:39], v[80:95]\n"
"ds_read_b64_tr_b16 a[108:109], v183 offset:18432\n"
"ds_read_b64_tr_b16 a[110:111], v183 offset:18944\n"
"ds_read_b64_tr_b16 a[80:81], v180 offset:20480\n"
"v_mfma_f32_32x32x16_bf16 v[96:111], a[100:103], a[36:39], v[96:111]\n"
"ds_read_b64_tr_b16 a[82:83], v180 offset:20992\n"
"buffer_load_dwordx4 v197, s[20:23], s78 offen offset:3072 lds\n"
"v_mfma_f32_32x32x16_bf16 v[112:127], a[104:107], a[36:39], v[112:127]\n"
"ds_read_b64_tr_b16 a[84:85], v181 offset:20480\n"
"ds_read_b64_tr_b16 a[86:87], v181 offset:20992\n"
"ds_read_b64_tr_b16 a[88:89], v182 offset:20480\n"
"s_waitcnt vmcnt(10)\n"
"v_cmp_ne_u32 vcc, v225, v226\n"
"s_nop 0\n"
"s_cbranch_vccnz L_fs_42_%=\n"
"L_fd_43_%=:\n"
"s_waitcnt lgkmcnt(5)\n"
"v_mfma_f32_32x32x16_bf16 v[128:143], a[108:111], a[36:39], v[128:143]\n"
"ds_read_b64_tr_b16 a[90:91], v182 offset:20992\n"
"buffer_load_dwordx4 a[112:115], v227, s[24:27], s41 offen offset:0 sc1\n"
"ds_read_b64_tr_b16 a[92:93], v183 offset:20480\n"
"s_waitcnt lgkmcnt(5)\n"
"v_mfma_f32_32x32x16_bf16 v[80:95], a[80:83], a[40:43], v[80:95]\n"
"ds_read_b64_tr_b16 a[94:95], v183 offset:20992\n"
"buffer_load_dwordx4 a[116:119], v227, s[24:27], s41 offen offset:1024 sc1\n"
"ds_read_b64_tr_b16 a[96:97], v180 offset:22528\n"
"s_waitcnt lgkmcnt(5)\n"
"v_mfma_f32_32x32x16_bf16 v[96:111], a[84:87], a[40:43], v[96:111]\n"
"ds_read_b64_tr_b16 a[98:99], v180 offset:23040\n"
"buffer_load_dwordx4 a[120:123], v227, s[24:27], s41 offen offset:2048 sc1\n"
"ds_read_b64_tr_b16 a[100:101], v181 offset:22528\n"
"s_waitcnt lgkmcnt(5)\n"
"v_mfma_f32_32x32x16_bf16 v[112:127], a[88:91], a[40:43], v[112:127]\n"
"ds_read_b64_tr_b16 a[102:103], v181 offset:23040\n"
"buffer_load_dwordx4 a[124:127], v227, s[24:27], s41 offen offset:3072 sc1\n"
"ds_read_b64_tr_b16 a[104:105], v182 offset:22528\n"
"s_waitcnt lgkmcnt(5)\n"
"v_mfma_f32_32x32x16_bf16 v[128:143], a[92:95], a[40:43], v[128:143]\n"
"ds_read_b64_tr_b16 a[106:107], v182 offset:23040\n"
"ds_read_b64_tr_b16 a[108:109], v183 offset:22528\n"
"ds_read_b64_tr_b16 a[110:111], v183 offset:23040\n"
"s_waitcnt lgkmcnt(6)\n"
"v_mfma_f32_32x32x16_bf16 v[80:95], a[96:99], a[44:47], v[80:95]\n"
"buffer_load_dwordx4 a[128:131], v227, s[24:27], s42 offen offset:0 sc1\n"
"ds_read_b64_tr_b16 a[80:81], v180 offset:32768\n"
"ds_read_b64_tr_b16 a[82:83], v180 offset:33280\n"
"s_waitcnt lgkmcnt(6)\n"
"v_mfma_f32_32x32x16_bf16 v[96:111], a[100:103], a[44:47], v[96:111]\n"
"ds_read_b64_tr_b16 a[84:85], v181 offset:32768\n"
"ds_read_b64_tr_b16 a[86:87], v181 offset:33280\n"
"ds_read_b64_tr_b16 a[88:89], v182 offset:32768\n"
"s_waitcnt lgkmcnt(5)\n"
"v_mfma_f32_32x32x16_bf16 v[112:127], a[104:107], a[44:47], v[112:127]\n"
"buffer_load_dwordx4 a[132:135], v227, s[24:27], s42 offen offset:1024 sc1\n"
"ds_read_b64_tr_b16 a[90:91], v182 offset:33280\n"
"ds_read_b64_tr_b16 a[92:93], v183 offset:32768\n"
"ds_read_b64_tr_b16 a[94:95], v183 offset:33280\n"
"v_mfma_f32_32x32x16_bf16 v[128:143], a[108:111], a[44:47], v[128:143]\n"
"ds_read_b64_tr_b16 a[96:97], v180 offset:34816\n"
"ds_read_b64_tr_b16 a[98:99], v180 offset:35328\n"
"ds_read_b64_tr_b16 a[100:101], v181 offset:34816\n"
"s_barrier\n"
"s_waitcnt lgkmcnt(7)\n"
"v_mfma_f32_32x32x16_bf16 v[80:95], a[80:83], a[48:51], v[80:95]\n"
"buffer_load_dwordx4 a[136:139], v227, s[24:27], s42 offen offset:2048 sc1\n"
"ds_read_b64_tr_b16 a[102:103], v181 offset:35328\n"
"ds_read_b64_tr_b16 a[104:105], v182 offset:34816\n"
"ds_read_b64_tr_b16 a[106:107], v182 offset:35328\n"
"v_mfma_f32_32x32x16_bf16 v[96:111], a[84:87], a[48:51], v[96:111]\n"
"buffer_load_dwordx4 a[140:143], v227, s[24:27], s42 offen offset:3072 sc1\n"
"ds_read_b64_tr_b16 a[108:109], v183 offset:34816\n"
"ds_read_b64_tr_b16 a[110:111], v183 offset:35328\n"
"s_waitcnt lgkmcnt(6)\n"
"v_mfma_f32_32x32x16_bf16 v[112:127], a[88:91], a[48:51], v[112:127]\n"
"s_add_u32 s78, s35, 1376256\n"
"s_add_u32 m0, s85, 81920\n"
"ds_read_b64_tr_b16 a[80:81], v180 offset:36864\n"
"ds_read_b64_tr_b16 a[82:83], v180 offset:37376\n"
"ds_read_b64_tr_b16 a[84:85], v181 offset:36864\n"
"v_mfma_f32_32x32x16_bf16 v[128:143], a[92:95], a[48:51], v[128:143]\n"
"buffer_load_dwordx4 a[144:147], v227, s[24:27], s43 offen offset:0 sc1\n"
"ds_read_b64_tr_b16 a[86:87], v181 offset:37376\n"
"ds_read_b64_tr_b16 a[88:89], v182 offset:36864\n"
"ds_read_b64_tr_b16 a[90:91], v182 offset:37376\n"
"v_mfma_f32_32x32x16_bf16 v[80:95], a[96:99], a[52:55], v[80:95]\n"
"ds_read_b64_tr_b16 a[92:93], v183 offset:36864\n"
"ds_read_b64_tr_b16 a[94:95], v183 offset:37376\n"
"s_waitcnt lgkmcnt(6)\n"
"v_mfma_f32_32x32x16_bf16 v[96:111], a[100:103], a[52:55], v[96:111]\n"
"buffer_load_dwordx4 a[148:151], v227, s[24:27], s43 offen offset:1024 sc1\n"
"ds_read_b64_tr_b16 a[96:97], v180 offset:38912\n"
"ds_read_b64_tr_b16 a[98:99], v180 offset:39424\n"
"v_mfma_f32_32x32x16_bf16 v[112:127], a[104:107], a[52:55], v[112:127]\n"
"ds_read_b64_tr_b16 a[100:101], v181 offset:38912\n"
"ds_read_b64_tr_b16 a[102:103], v181 offset:39424\n"
"v_mfma_f32_32x32x16_bf16 v[128:143], a[108:111], a[52:55], v[128:143]\n"
"buffer_load_dwordx4 a[152:155], v227, s[24:27], s43 offen offset:2048 sc1\n"
"ds_read_b64_tr_b16 a[104:105], v182 offset:38912\n"
"ds_read_b64_tr_b16 a[106:107], v182 offset:39424\n"
"v_mfma_f32_32x32x16_bf16 v[80:95], a[80:83], a[56:59], v[80:95]\n"
"ds_read_b64_tr_b16 a[108:109], v183 offset:38912\n"
"ds_read_b64_tr_b16 a[110:111], v183 offset:39424\n"
"s_waitcnt lgkmcnt(4)\n"
"v_mfma_f32_32x32x16_bf16 v[96:111], a[84:87], a[56:59], v[96:111]\n"
"buffer_load_dwordx4 a[156:159], v227, s[24:27], s43 offen offset:3072 sc1\n"
"ds_read_b64_tr_b16 a[80:81], v180 offset:49152\n"
"ds_read_b64_tr_b16 a[82:83], v180 offset:49664\n"
"v_mfma_f32_32x32x16_bf16 v[112:127], a[88:91], a[56:59], v[112:127]\n"
"buffer_load_dwordx4 a[160:163], v227, s[24:27], s44 offen offset:0 sc1\n"
"ds_read_b64_tr_b16 a[84:85], v180 offset:51200\n"
"ds_read_b64_tr_b16 a[86:87], v180 offset:51712\n"
"v_mfma_f32_32x32x16_bf16 v[128:143], a[92:95], a[56:59], v[128:143]\n"
"ds_read_b64_tr_b16 a[88:89], v180 offset:53248\n"
"ds_read_b64_tr_b16 a[90:91], v180 offset:53760\n"
"v_mfma_f32_32x32x16_bf16 v[80:95], a[96:99], a[60:63], v[80:95]\n"
"buffer_load_dwordx4 a[164:167], v227, s[24:27], s44 offen offset:1024 sc1\n"
"ds_read_b64_tr_b16 a[92:93], v180 offset:55296\n"
"ds_read_b64_tr_b16 a[94:95], v180 offset:55808\n"
"v_mfma_f32_32x32x16_bf16 v[96:111], a[100:103], a[60:63], v[96:111]\n"
"ds_read_b64_tr_b16 a[96:97], v181 offset:49152\n"
"ds_read_b64_tr_b16 a[98:99], v181 offset:49664\n"
"s_waitcnt lgkmcnt(4)\n"
"v_mfma_f32_32x32x16_bf16 v[112:127], a[104:107], a[60:63], v[112:127]\n"
"buffer_load_dwordx4 a[168:171], v227, s[24:27], s44 offen offset:2048 sc1\n"
"ds_read_b64_tr_b16 a[100:101], v181 offset:51200\n"
"ds_read_b64_tr_b16 a[102:103], v181 offset:51712\n"
"v_mfma_f32_32x32x16_bf16 v[128:143], a[108:111], a[60:63], v[128:143]\n"
"ds_read_b64_tr_b16 a[104:105], v181 offset:53248\n"
"ds_read_b64_tr_b16 a[106:107], v181 offset:53760\n"
"s_barrier\n"
"v_mfma_f32_32x32x16_bf16 v[80:95], a[80:83], a[64:67], v[80:95]\n"
"buffer_load_dwordx4 a[172:175], v227, s[24:27], s44 offen offset:3072 sc1\n"
"ds_read_b64_tr_b16 a[108:109], v181 offset:55296\n"
"ds_read_b64_tr_b16 a[110:111], v181 offset:55808\n"
"v_mfma_f32_32x32x16_bf16 v[80:95], a[84:87], a[68:71], v[80:95]\n"
"buffer_load_dwordx4 a[176:179], v227, s[24:27], s45 offen offset:0 sc1\n"
"ds_read_b64_tr_b16 a[80:81], v182 offset:49152\n"
"ds_read_b64_tr_b16 a[82:83], v182 offset:49664\n"
"v_mfma_f32_32x32x16_bf16 v[80:95], a[88:91], a[72:75], v[80:95]\n"
"ds_read_b64_tr_b16 a[84:85], v182 offset:51200\n"
"ds_read_b64_tr_b16 a[86:87], v182 offset:51712\n"
"s_waitcnt lgkmcnt(4)\n"
"v_mfma_f32_32x32x16_bf16 v[80:95], a[92:95], a[76:79], v[80:95]\n"
"buffer_load_dwordx4 a[180:183], v227, s[24:27], s45 offen offset:1024 sc1\n"
"ds_read_b64_tr_b16 a[88:89], v182 offset:53248\n"
"ds_read_b64_tr_b16 a[90:91], v182 offset:53760\n"
"v_mfma_f32_32x32x16_bf16 v[96:111], a[96:99], a[64:67], v[96:111]\n"
"ds_read_b64_tr_b16 a[92:93], v182 offset:55296\n"
"ds_read_b64_tr_b16 a[94:95], v182 offset:55808\n"
"v_mfma_f32_32x32x16_bf16 v[96:111], a[100:103], a[68:71], v[96:111]\n"
"s_nop 4\n"
"v_mul_f32 v80, v80, v88\n"
"v_mul_f32 v81, v81, v89\n"
"v_cvt_pk_bf16_f32 v80, v80, v81\n"
"v_mul_f32 v82, v82, v90\n"
"v_mfma_f32_32x32x16_bf16 v[96:111], a[104:107], a[72:75], v[96:111]\n"
"v_mul_f32 v83, v83, v91\n"
"v_cvt_pk_bf16_f32 v81, v82, v83\n"
"v_mul_f32 v84, v84, v92\n"
"v_mul_f32 v85, v85, v93\n"
"v_cvt_pk_bf16_f32 v82, v84, v85\n"
"v_mul_f32 v86, v86, v94\n"
"v_mfma_f32_32x32x16_bf16 v[96:111], a[108:111], a[76:79], v[96:111]\n"
"ds_read_b64_tr_b16 a[96:97], v183 offset:49152\n"
"ds_read_b64_tr_b16 a[98:99], v183 offset:49664\n"
"v_mul_f32 v87, v87, v95\n"
"v_cvt_pk_bf16_f32 v83, v86, v87\n"
"buffer_store_dwordx4 v[80:83], v227, s[24:27], s48 offen offset:0 sc1\n"
"s_waitcnt lgkmcnt(2)\n"
"v_mfma_f32_32x32x16_bf16 v[112:127], a[80:83], a[64:67], v[112:127]\n"
"ds_read_b64_tr_b16 a[100:101], v183 offset:51200\n"
"ds_read_b64_tr_b16 a[102:103], v183 offset:51712\n"
"ds_read_b64_tr_b16 a[104:105], v183 offset:53248\n"
"v_mfma_f32_32x32x16_bf16 v[112:127], a[84:87], a[68:71], v[112:127]\n"
"ds_read_b64_tr_b16 a[106:107], v183 offset:53760\n"
"ds_read_b64_tr_b16 a[108:109], v183 offset:55296\n"
"v_mul_f32 v96, v96, v104\n"
"v_mul_f32 v97, v97, v105\n"
"v_cvt_pk_bf16_f32 v96, v96, v97\n"
"v_mul_f32 v98, v98, v106\n"
"v_mfma_f32_32x32x16_bf16 v[112:127], a[88:91], a[72:75], v[112:127]\n"
"ds_read_b64_tr_b16 a[110:111], v183 offset:55808\n"
"ds_read_b64_tr_b16 a[80:81], v189 offset:0\n"
"v_mul_f32 v99, v99, v107\n"
"v_cvt_pk_bf16_f32 v97, v98, v99\n"
"v_mul_f32 v100, v100, v108\n"
"v_mul_f32 v101, v101, v109\n"
"v_mfma_f32_32x32x16_bf16 v[112:127], a[92:95], a[76:79], v[112:127]\n"
"ds_read_b64_tr_b16 a[82:83], v189 offset:512\n"
"ds_read_b64_tr_b16 a[84:85], v188 offset:0\n"
"v_cvt_pk_bf16_f32 v98, v100, v101\n"
"v_mul_f32 v102, v102, v110\n"
"v_mul_f32 v103, v103, v111\n"
"s_waitcnt lgkmcnt(5)\n"
"v_mfma_f32_32x32x16_bf16 v[128:143], a[96:99], a[64:67], v[128:143]\n"
"ds_read_b64_tr_b16 a[86:87], v188 offset:512\n"
"ds_read_b64_tr_b16 a[88:89], v189 offset:2048\n"
"v_cvt_pk_bf16_f32 v99, v102, v103\n"
"buffer_store_dwordx4 v[96:99], v227, s[24:27], s48 offen offset:1024 sc1\n"
"v_mfma_f32_32x32x16_bf16 v[128:143], a[100:103], a[68:71], v[128:143]\n"
"ds_read_b64_tr_b16 a[90:91], v189 offset:2560\n"
"ds_read_b64_tr_b16 a[92:93], v188 offset:2048\n"
"v_mul_f32 v112, v112, v120\n"
"v_mul_f32 v113, v113, v121\n"
"v_cvt_pk_bf16_f32 v112, v112, v113\n"
"v_mul_f32 v114, v114, v122\n"
"v_mfma_f32_32x32x16_bf16 v[128:143], a[104:107], a[72:75], v[128:143]\n"
"ds_read_b64_tr_b16 a[94:95], v188 offset:2560\n"
"ds_read_b64_tr_b16 a[96:97], v189 offset:4096\n"
"v_mul_f32 v115, v115, v123\n"
"v_cvt_pk_bf16_f32 v113, v114, v115\n"
"v_mul_f32 v116, v116, v124\n"
"s_waitcnt lgkmcnt(5)\n"
"v_mfma_f32_32x32x16_bf16 v[128:143], a[108:111], a[76:79], v[128:143]\n"
"ds_read_b64_tr_b16 a[98:99], v189 offset:4608\n"
"ds_read_b64_tr_b16 a[100:101], v188 offset:4096\n"
"v_mul_f32 v117, v117, v125\n"
"v_cvt_pk_bf16_f32 v114, v116, v117\n"
"v_mul_f32 v118, v118, v126\n"
"v_mul_f32 v119, v119, v127\n"
"s_barrier\n"
"s_waitcnt vmcnt(11)\n"
"v_mfma_f32_32x32x16_bf16 a[224:239], a[80:83], a[112:115], 0\n"
"ds_read_b64_tr_b16 a[102:103], v188 offset:4608\n"
"ds_read_b64_tr_b16 a[104:105], v189 offset:6144\n"
"v_cvt_pk_bf16_f32 v115, v118, v119\n"
"buffer_store_dwordx4 v[112:115], v227, s[24:27], s48 offen offset:2048 sc1\n"
"v_mfma_f32_32x32x16_bf16 v[160:175], a[84:87], a[112:115], 0\n"
"ds_read_b64_tr_b16 a[106:107], v189 offset:6656\n"
"ds_read_b64_tr_b16 a[108:109], v188 offset:6144\n"
"v_mul_f32 v128, v128, v136\n"
"v_mul_f32 v129, v129, v137\n"
"v_cvt_pk_bf16_f32 v128, v128, v129\n"
"s_waitcnt lgkmcnt(5)\n"
"v_mfma_f32_32x32x16_bf16 a[224:239], a[88:91], a[116:119], a[224:239]\n"
"ds_read_b64_tr_b16 a[110:111], v188 offset:6656\n"
"ds_read_b64_tr_b16 a[80:81], v189 offset:16384\n"
"v_mul_f32 v130, v130, v138\n"
"v_mul_f32 v131, v131, v139\n"
"v_cvt_pk_bf16_f32 v129, v130, v131\n"
"v_mul_f32 v132, v132, v140\n"
"buffer_load_dwordx4 a[184:187], v227, s[24:27], s45 offen offset:2048 sc1\n"
"v_mfma_f32_32x32x16_bf16 v[160:175], a[92:95], a[116:119], v[160:175]\n"
"ds_read_b64_tr_b16 a[82:83], v189 offset:16896\n"
"ds_read_b64_tr_b16 a[84:85], v188 offset:16384\n"
"v_mul_f32 v133, v133, v141\n"
"v_cvt_pk_bf16_f32 v130, v132, v133\n"
"v_mul_f32 v134, v134, v142\n"
"v_mul_f32 v135, v135, v143\n"
"buffer_load_dwordx4 a[188:191], v227, s[24:27], s45 offen offset:3072 sc1\n"
"v_mfma_f32_32x32x16_bf16 a[224:239], a[96:99], a[120:123], a[224:239]\n"
"ds_read_b64_tr_b16 a[86:87], v188 offset:16896\n"
"ds_read_b64_tr_b16 a[88:89], v189 offset:18432\n"
"v_cvt_pk_bf16_f32 v131, v134, v135\n"
"buffer_store_dwordx4 v[128:131], v227, s[24:27], s48 offen offset:3072 sc1\n"
"s_waitcnt lgkmcnt(5)\n"
"v_mfma_f32_32x32x16_bf16 v[160:175], a[100:103], a[120:123], v[160:175]\n"
"ds_write_b128 v243, v[16:19] offset:49152\n"
"v_mfma_f32_32x32x16_bf16 a[224:239], a[104:107], a[124:127], a[224:239]\n"
"ds_write_b128 v243, v[20:23] offset:50176\n"
"v_mfma_f32_32x32x16_bf16 v[160:175], a[108:111], a[124:127], v[160:175]\n"
"ds_read_b64_tr_b16 a[90:91], v189 offset:18944\n"
"ds_read_b64_tr_b16 a[92:93], v188 offset:18432\n"
"ds_read_b64_tr_b16 a[94:95], v188 offset:18944\n"
"s_barrier\n"
"s_waitcnt lgkmcnt(6)\n"
"v_mfma_f32_32x32x16_bf16 a[224:239], a[80:83], a[128:131], a[224:239]\n"
"ds_write_b128 v243, v[24:27] offset:51200\n"
"v_mfma_f32_32x32x16_bf16 v[160:175], a[84:87], a[128:131], v[160:175]\n"
"ds_read_b64_tr_b16 a[96:97], v189 offset:20480\n"
"ds_read_b64_tr_b16 a[98:99], v189 offset:20992\n"
"buffer_load_dwordx4 a[192:195], v227, s[24:27], s46 offen offset:0 sc1\n"
"s_waitcnt lgkmcnt(3)\n"
"v_mfma_f32_32x32x16_bf16 a[224:239], a[88:91], a[132:135], a[224:239]\n"
"ds_read_b64_tr_b16 a[100:101], v188 offset:20480\n"
"ds_read_b64_tr_b16 a[102:103], v188 offset:20992\n"
"ds_read_b64_tr_b16 a[104:105], v189 offset:22528\n"
"v_mfma_f32_32x32x16_bf16 v[160:175], a[92:95], a[132:135], v[160:175]\n"
"ds_read_b64_tr_b16 a[106:107], v189 offset:23040\n"
"buffer_load_dwordx4 v194, s[20:23], s78 offen offset:0 lds\n"
"s_waitcnt lgkmcnt(4)\n"
"v_mfma_f32_32x32x16_bf16 a[224:239], a[96:99], a[136:139], a[224:239]\n"
"ds_read_b64_tr_b16 a[108:109], v188 offset:22528\n"
"ds_read_b64_tr_b16 a[110:111], v188 offset:23040\n"
"ds_read_b64_tr_b16 a[80:81], v189 offset:32768\n"
"s_waitcnt lgkmcnt(5)\n"
"v_mfma_f32_32x32x16_bf16 v[160:175], a[100:103], a[136:139], v[160:175]\n"
"ds_read_b64_tr_b16 a[82:83], v189 offset:33280\n"
"buffer_load_dwordx4 v195, s[20:23], s78 offen offset:1024 lds\n"
"s_waitcnt lgkmcnt(4)\n"
"v_mfma_f32_32x32x16_bf16 a[224:239], a[104:107], a[140:143], a[224:239]\n"
"ds_read_b64_tr_b16 a[84:85], v188 offset:32768\n"
"ds_read_b64_tr_b16 a[86:87], v188 offset:33280\n"
"buffer_load_dwordx4 v196, s[20:23], s78 offen offset:2048 lds\n"
"s_waitcnt lgkmcnt(4)\n"
"v_mfma_f32_32x32x16_bf16 v[160:175], a[108:111], a[140:143], v[160:175]\n"
"ds_read_b64_tr_b16 a[88:89], v189 offset:34816\n"
"ds_read_b64_tr_b16 a[90:91], v189 offset:35328\n"
"ds_read_b64_tr_b16 a[92:93], v188 offset:34816\n"
"s_barrier\n"
"s_waitcnt lgkmcnt(5)\n"
"v_mfma_f32_32x32x16_bf16 a[224:239], a[80:83], a[144:147], a[224:239]\n"
"ds_read_b64_tr_b16 a[94:95], v188 offset:35328\n"
"buffer_load_dwordx4 v197, s[20:23], s78 offen offset:3072 lds\n"
"s_waitcnt lgkmcnt(4)\n"
"v_mfma_f32_32x32x16_bf16 v[160:175], a[84:87], a[144:147], v[160:175]\n"
"ds_read_b64_tr_b16 a[96:97], v189 offset:36864\n"
"ds_read_b64_tr_b16 a[98:99], v189 offset:37376\n"
"s_add_u32 s78, s35, 57344\n"
"s_add_u32 m0, s85, 98304\n"
"ds_read_b64_tr_b16 a[100:101], v188 offset:36864\n"
"s_waitcnt vmcnt(11)\n"
"s_waitcnt lgkmcnt(5)\n"
"v_mfma_f32_32x32x16_bf16 a[224:239], a[88:91], a[148:151], a[224:239]\n"
"ds_read_b64_tr_b16 a[102:103], v188 offset:37376\n"
"buffer_load_dwordx4 a[196:199], v227, s[24:27], s46 offen offset:1024 sc1\n"
"ds_read_b64_tr_b16 a[104:105], v189 offset:38912\n"
"s_waitcnt lgkmcnt(5)\n"
"v_mfma_f32_32x32x16_bf16 v[160:175], a[92:95], a[148:151], v[160:175]\n"
"ds_read_b64_tr_b16 a[106:107], v189 offset:39424\n"
"ds_read_b64_tr_b16 a[108:109], v188 offset:38912\n"
"ds_read_b64_tr_b16 a[110:111], v188 offset:39424\n"
"s_waitcnt lgkmcnt(6)\n"
"v_mfma_f32_32x32x16_bf16 a[224:239], a[96:99], a[152:155], a[224:239]\n"
"buffer_load_dwordx4 a[200:203], v227, s[24:27], s46 offen offset:2048 sc1\n"
"ds_read_b64_tr_b16 a[80:81], v189 offset:49152\n"
"ds_read_b64_tr_b16 a[82:83], v189 offset:49664\n"
"s_waitcnt lgkmcnt(6)\n"
"v_mfma_f32_32x32x16_bf16 v[160:175], a[100:103], a[152:155], v[160:175]\n"
"buffer_load_dwordx4 a[204:207], v227, s[24:27], s46 offen offset:3072 sc1\n"
"ds_read_b64_tr_b16 a[84:85], v188 offset:49152\n"
"ds_read_b64_tr_b16 a[86:87], v188 offset:49664\n"
"s_waitcnt lgkmcnt(4)\n"
"v_mfma_f32_32x32x16_bf16 a[224:239], a[104:107], a[156:159], a[224:239]\n"
"ds_write_b128 v243, v[28:31] offset:52224\n"
"v_mfma_f32_32x32x16_bf16 v[160:175], a[108:111], a[156:159], v[160:175]\n"
"ds_read_b64_tr_b16 a[88:89], v189 offset:51200\n"
"ds_read_b64_tr_b16 a[90:91], v189 offset:51712\n"
"buffer_load_dwordx4 a[208:211], v227, s[24:27], s47 offen offset:0 sc1\n"
"s_barrier\n"
"s_waitcnt lgkmcnt(3)\n"
"v_mfma_f32_32x32x16_bf16 a[224:239], a[80:83], a[160:163], a[224:239]\n"
"ds_read_b64_tr_b16 a[92:93], v188 offset:51200\n"
"ds_read_b64_tr_b16 a[94:95], v188 offset:51712\n"
"ds_read_b64_tr_b16 a[96:97], v189 offset:53248\n"
"v_mfma_f32_32x32x16_bf16 v[160:175], a[84:87], a[160:163], v[160:175]\n"
"ds_read_b64_tr_b16 a[98:99], v189 offset:53760\n"
"buffer_load_dwordx4 v194, s[20:23], s78 offen offset:0 lds\n"
"s_waitcnt lgkmcnt(4)\n"
"v_mfma_f32_32x32x16_bf16 a[224:239], a[88:91], a[164:167], a[224:239]\n"
"ds_read_b64_tr_b16 a[100:101], v188 offset:53248\n"
"ds_read_b64_tr_b16 a[102:103], v188 offset:53760\n"
"ds_read_b64_tr_b16 a[104:105], v189 offset:55296\n"
"s_waitcnt lgkmcnt(5)\n"
"v_mfma_f32_32x32x16_bf16 v[160:175], a[92:95], a[164:167], v[160:175]\n"
"ds_read_b64_tr_b16 a[106:107], v189 offset:55808\n"
"buffer_load_dwordx4 v195, s[20:23], s78 offen offset:1024 lds\n"
"s_waitcnt lgkmcnt(4)\n"
"v_mfma_f32_32x32x16_bf16 a[224:239], a[96:99], a[168:171], a[224:239]\n"
"ds_read_b64_tr_b16 a[108:109], v188 offset:55296\n"
"ds_read_b64_tr_b16 a[110:111], v188 offset:55808\n"
"buffer_load_dwordx4 v196, s[20:23], s78 offen offset:2048 lds\n"
"s_waitcnt lgkmcnt(4)\n"
"v_mfma_f32_32x32x16_bf16 v[160:175], a[100:103], a[168:171], v[160:175]\n"
"ds_read_b64_tr_b16 a[80:81], v193 offset:0\n"
"ds_read_b64_tr_b16 a[82:83], v193 offset:512\n"
"ds_read_b64_tr_b16 a[84:85], v192 offset:0\n"
"s_waitcnt lgkmcnt(5)\n"
"v_mfma_f32_32x32x16_bf16 a[224:239], a[104:107], a[172:175], a[224:239]\n"
"ds_read_b64_tr_b16 a[86:87], v192 offset:512\n"
"buffer_load_dwordx4 v197, s[20:23], s78 offen offset:3072 lds\n"
"s_waitcnt lgkmcnt(4)\n"
"v_mfma_f32_32x32x16_bf16 v[160:175], a[108:111], a[172:175], v[160:175]\n"
"ds_read_b64_tr_b16 a[88:89], v193 offset:2048\n"
"ds_read_b64_tr_b16 a[90:91], v193 offset:2560\n"
"ds_read_b64_tr_b16 a[92:93], v192 offset:2048\n"
"s_barrier\n"
"s_waitcnt lgkmcnt(5)\n"
"v_mfma_f32_32x32x16_bf16 a[224:239], a[80:83], a[176:179], a[224:239]\n"
"ds_read_b64_tr_b16 a[94:95], v192 offset:2560\n"
"buffer_load_dwordx4 a[212:215], v227, s[24:27], s47 offen offset:1024 sc1\n"
"ds_read_b64_tr_b16 a[96:97], v193 offset:4096\n"
"s_waitcnt lgkmcnt(5)\n"
"v_mfma_f32_32x32x16_bf16 v[160:175], a[84:87], a[176:179], v[160:175]\n"
"ds_read_b64_tr_b16 a[98:99], v193 offset:4608\n"
"buffer_load_dwordx4 a[216:219], v227, s[24:27], s47 offen offset:2048 sc1\n"
"ds_read_b64_tr_b16 a[100:101], v192 offset:4096\n"
"s_waitcnt lgkmcnt(5)\n"
"v_mfma_f32_32x32x16_bf16 a[224:239], a[88:91], a[180:183], a[224:239]\n"
"ds_read_b64_tr_b16 a[102:103], v192 offset:4608\n"
"s_add_u32 s78, s35, 1204224\n"
"s_add_u32 m0, s85, 49152\n"
"ds_read_b64_tr_b16 a[104:105], v193 offset:6144\n"
"ds_read_b64_tr_b16 a[106:107], v193 offset:6656\n"
"s_waitcnt lgkmcnt(6)\n"
"v_mfma_f32_32x32x16_bf16 v[160:175], a[92:95], a[180:183], v[160:175]\n"
"buffer_load_dwordx4 a[220:223], v227, s[24:27], s47 offen offset:3072 sc1\n"
"ds_read_b64_tr_b16 a[108:109], v192 offset:6144\n"
"ds_read_b64_tr_b16 a[110:111], v192 offset:6656\n"
"s_waitcnt vmcnt(17)\n"
"s_waitcnt lgkmcnt(6)\n"
"v_mfma_f32_32x32x16_bf16 a[224:239], a[96:99], a[184:187], a[224:239]\n"
"ds_write_b128 v242, v[32:35] offset:0\n"
"s_waitcnt lgkmcnt(3)\n"
"v_mfma_f32_32x32x16_bf16 v[160:175], a[100:103], a[184:187], v[160:175]\n"
"ds_read_b64_tr_b16 a[80:81], v191 offset:0\n"
"ds_read_b64_tr_b16 a[82:83], v191 offset:512\n"
"buffer_load_dwordx4 v[80:83], v227, s[24:27], s88 offen offset:0 sc1\n"
"ds_read_b64_tr_b16 a[84:85], v190 offset:0\n"
"v_mfma_f32_32x32x16_bf16 a[224:239], a[104:107], a[188:191], a[224:239]\n"
"ds_read_b64_tr_b16 a[86:87], v190 offset:512\n"
"ds_read_b64_tr_b16 a[88:89], v191 offset:2048\n"
"ds_read_b64_tr_b16 a[90:91], v191 offset:2560\n"
"s_waitcnt lgkmcnt(7)\n"
"v_mfma_f32_32x32x16_bf16 v[160:175], a[108:111], a[188:191], v[160:175]\n"
"buffer_load_dwordx4 v[84:87], v227, s[24:27], s88 offen offset:1024 sc1\n"
"ds_read_b64_tr_b16 a[92:93], v190 offset:2048\n"
"ds_read_b64_tr_b16 a[94:95], v190 offset:2560\n"
"s_waitcnt vmcnt(13)\n"
"s_barrier\n"
"s_waitcnt lgkmcnt(6)\n"
"v_mfma_f32_32x32x16_bf16 a[224:239], a[80:83], a[192:195], a[224:239]\n"
"ds_write_b128 v242, v[36:39] offset:1024\n"
"s_waitcnt lgkmcnt(3)\n"
"v_mfma_f32_32x32x16_bf16 v[160:175], a[84:87], a[192:195], v[160:175]\n"
"ds_read_b64_tr_b16 a[96:97], v191 offset:4096\n"
"ds_read_b64_tr_b16 a[98:99], v191 offset:4608\n"
"buffer_load_dwordx4 v[88:91], v227, s[24:27], s88 offen offset:2048 sc1\n"
"ds_read_b64_tr_b16 a[100:101], v190 offset:4096\n"
"s_waitcnt vmcnt(13)\n"
"v_mfma_f32_32x32x16_bf16 a[224:239], a[88:91], a[196:199], a[224:239]\n"
"ds_read_b64_tr_b16 a[102:103], v190 offset:4608\n"
"buffer_load_dwordx4 v[92:95], v227, s[24:27], s88 offen offset:3072 sc1\n"
"ds_read_b64_tr_b16 a[104:105], v191 offset:6144\n"
"s_waitcnt lgkmcnt(6)\n"
"v_mfma_f32_32x32x16_bf16 v[160:175], a[92:95], a[196:199], v[160:175]\n"
"ds_read_b64_tr_b16 a[106:107], v191 offset:6656\n"
"ds_read_b64_tr_b16 a[108:109], v190 offset:6144\n"
"ds_read_b64_tr_b16 a[110:111], v190 offset:6656\n"
"s_waitcnt vmcnt(13)\n"
"s_waitcnt lgkmcnt(6)\n"
"v_mfma_f32_32x32x16_bf16 a[224:239], a[96:99], a[200:203], a[224:239]\n"
"buffer_load_dwordx4 v[96:99], v227, s[24:27], s89 offen offset:0 sc1\n"
"ds_read_b64_tr_b16 a[80:81], v191 offset:16384\n"
"ds_read_b64_tr_b16 a[82:83], v191 offset:16896\n"
"s_waitcnt lgkmcnt(6)\n"
"v_mfma_f32_32x32x16_bf16 v[160:175], a[100:103], a[200:203], v[160:175]\n"
"ds_write_b128 v242, v[40:43] offset:2048\n"
"s_waitcnt vmcnt(13)\n"
"s_waitcnt lgkmcnt(3)\n"
"v_mfma_f32_32x32x16_bf16 a[224:239], a[104:107], a[204:207], a[224:239]\n"
"ds_read_b64_tr_b16 a[84:85], v190 offset:16384\n"
"ds_read_b64_tr_b16 a[86:87], v190 offset:16896\n"
"buffer_load_dwordx4 v[100:103], v227, s[24:27], s89 offen offset:1024 sc1\n"
"ds_read_b64_tr_b16 a[88:89], v191 offset:18432\n"
"v_mfma_f32_32x32x16_bf16 v[160:175], a[108:111], a[204:207], v[160:175]\n"
"ds_read_b64_tr_b16 a[90:91], v191 offset:18944\n"
"ds_read_b64_tr_b16 a[92:93], v190 offset:18432\n"
"ds_read_b64_tr_b16 a[94:95], v190 offset:18944\n"
"s_waitcnt vmcnt(9)\n"
"s_barrier\n"
"s_waitcnt lgkmcnt(7)\n"
"v_mfma_f32_32x32x16_bf16 a[224:239], a[80:83], a[208:211], a[224:239]\n"
"buffer_load_dwordx4 v[104:107], v227, s[24:27], s89 offen offset:2048 sc1\n"
"ds_read_b64_tr_b16 a[96:97], v191 offset:20480\n"
"ds_read_b64_tr_b16 a[98:99], v191 offset:20992\n"
"s_waitcnt lgkmcnt(6)\n"
"v_mfma_f32_32x32x16_bf16 v[160:175], a[84:87], a[208:211], v[160:175]\n"
"ds_write_b128 v242, v[44:47] offset:3072\n"
"s_waitcnt vmcnt(9)\n"
"s_waitcnt lgkmcnt(3)\n"
"v_mfma_f32_32x32x16_bf16 a[224:239], a[88:91], a[212:215], a[224:239]\n"
"ds_read_b64_tr_b16 a[100:101], v190 offset:20480\n"
"ds_read_b64_tr_b16 a[102:103], v190 offset:20992\n"
"buffer_load_dwordx4 v[108:111], v227, s[24:27], s89 offen offset:3072 sc1\n"
"ds_read_b64_tr_b16 a[104:105], v191 offset:22528\n"
"v_mfma_f32_32x32x16_bf16 v[160:175], a[92:95], a[212:215], v[160:175]\n"
"ds_read_b64_tr_b16 a[106:107], v191 offset:23040\n"
"buffer_load_dwordx4 v[112:115], v227, s[24:27], s90 offen offset:0 sc1\n"
"ds_read_b64_tr_b16 a[108:109], v190 offset:22528\n"
"s_waitcnt vmcnt(10)\n"
"s_waitcnt lgkmcnt(6)\n"
"v_mfma_f32_32x32x16_bf16 a[224:239], a[96:99], a[216:219], a[224:239]\n"
"ds_read_b64_tr_b16 a[110:111], v190 offset:23040\n"
"ds_read_b64_tr_b16 a[80:81], v191 offset:32768\n"
"ds_read_b64_tr_b16 a[82:83], v191 offset:33280\n"
"s_waitcnt lgkmcnt(6)\n"
"v_mfma_f32_32x32x16_bf16 v[160:175], a[100:103], a[216:219], v[160:175]\n"
"buffer_load_dwordx4 v[116:119], v227, s[24:27], s90 offen offset:1024 sc1\n"
"ds_read_b64_tr_b16 a[84:85], v190 offset:32768\n"
"ds_read_b64_tr_b16 a[86:87], v190 offset:33280\n"
"s_waitcnt vmcnt(10)\n"
"s_waitcnt lgkmcnt(6)\n"
"v_mfma_f32_32x32x16_bf16 a[224:239], a[104:107], a[220:223], a[224:239]\n"
"ds_write_b128 v242, v[48:51] offset:16384\n"
"s_waitcnt lgkmcnt(3)\n"
"v_mfma_f32_32x32x16_bf16 v[160:175], a[108:111], a[220:223], v[160:175]\n"
"ds_read_b64_tr_b16 a[88:89], v191 offset:34816\n"
"ds_read_b64_tr_b16 a[90:91], v191 offset:35328\n"
"buffer_load_dwordx4 v[120:123], v227, s[24:27], s90 offen offset:2048 sc1\n"
"ds_read_b64_tr_b16 a[92:93], v190 offset:34816\n"
"s_waitcnt lgkmcnt(3)\n"
"s_barrier\n"
"s_waitcnt vmcnt(10)\n"
"v_mfma_f32_32x32x16_bf16 a[240:255], a[80:83], v[80:83], a[224:239]\n"
"ds_read_b64_tr_b16 a[94:95], v190 offset:35328\n"
"ds_read_b64_tr_b16 a[96:97], v191 offset:36864\n"
"ds_read_b64_tr_b16 a[98:99], v191 offset:37376\n"
"v_mfma_f32_32x32x16_bf16 v[16:31], a[84:87], v[80:83], v[160:175]\n"
"buffer_load_dwordx4 v[124:127], v227, s[24:27], s90 offen offset:3072 sc1\n"
"ds_read_b64_tr_b16 a[100:101], v190 offset:36864\n"
"ds_read_b64_tr_b16 a[102:103], v190 offset:37376\n"
"s_waitcnt vmcnt(10)\n"
"s_waitcnt lgkmcnt(6)\n"
"v_mfma_f32_32x32x16_bf16 a[240:255], a[88:91], v[84:87], a[240:255]\n"
"buffer_load_dwordx4 v[128:131], v227, s[24:27], s91 offen offset:0 sc1\n"
"ds_read_b64_tr_b16 a[104:105], v191 offset:38912\n"
"ds_read_b64_tr_b16 a[106:107], v191 offset:39424\n"
"s_waitcnt lgkmcnt(4)\n"
"v_mfma_f32_32x32x16_bf16 v[16:31], a[92:95], v[84:87], v[16:31]\n"
"ds_write_b128 v242, v[52:55] offset:17408\n"
"s_waitcnt vmcnt(10)\n"
"v_mfma_f32_32x32x16_bf16 a[240:255], a[96:99], v[88:91], a[240:255]\n"
"ds_read_b64_tr_b16 a[108:109], v190 offset:38912\n"
"ds_read_b64_tr_b16 a[110:111], v190 offset:39424\n"
"buffer_load_dwordx4 v[132:135], v227, s[24:27], s91 offen offset:1024 sc1\n"
"s_waitcnt lgkmcnt(3)\n"
"v_mfma_f32_32x32x16_bf16 v[16:31], a[100:103], v[88:91], v[16:31]\n"
"ds_read_b64_tr_b16 a[80:81], v191 offset:49152\n"
"ds_read_b64_tr_b16 a[82:83], v191 offset:49664\n"
"ds_read_b64_tr_b16 a[84:85], v190 offset:49152\n"
"s_waitcnt vmcnt(10)\n"
"v_mfma_f32_32x32x16_bf16 a[240:255], a[104:107], v[92:95], a[240:255]\n"
"ds_read_b64_tr_b16 a[86:87], v190 offset:49664\n"
"buffer_load_dwordx4 v[136:139], v227, s[24:27], s91 offen offset:2048 sc1\n"
"ds_read_b64_tr_b16 a[88:89], v191 offset:51200\n"
"s_waitcnt lgkmcnt(5)\n"
"v_mfma_f32_32x32x16_bf16 v[16:31], a[108:111], v[92:95], v[16:31]\n"
"ds_read_b64_tr_b16 a[90:91], v191 offset:51712\n"
"ds_write_b128 v242, v[56:59] offset:18432\n"
"s_waitcnt lgkmcnt(0)\n"
"s_barrier\n"
"s_waitcnt vmcnt(10)\n"
"v_mfma_f32_32x32x16_bf16 a[240:255], a[80:83], v[96:99], a[240:255]\n"
"ds_read_b64_tr_b16 a[92:93], v190 offset:51200\n"
"ds_read_b64_tr_b16 a[94:95], v190 offset:51712\n"
"buffer_load_dwordx4 v[140:143], v227, s[24:27], s91 offen offset:3072 sc1\n"
"ds_read_b64_tr_b16 a[96:97], v191 offset:53248\n"
"v_mfma_f32_32x32x16_bf16 v[16:31], a[84:87], v[96:99], v[16:31]\n"
"ds_read_b64_tr_b16 a[98:99], v191 offset:53760\n"
"ds_write_b128 v242, v[60:63] offset:19456\n"
"s_waitcnt vmcnt(10)\n"
"v_mfma_f32_32x32x16_bf16 a[240:255], a[88:91], v[100:103], a[240:255]\n"
"ds_read_b64_tr_b16 a[100:101], v190 offset:53248\n"
"ds_read_b64_tr_b16 a[102:103], v190 offset:53760\n"
"buffer_load_dwordx4 v[144:147], v227, s[24:27], s92 offen offset:0 sc1\n"
"s_waitcnt lgkmcnt(5)\n"
"v_mfma_f32_32x32x16_bf16 v[16:31], a[92:95], v[100:103], v[16:31]\n"
"ds_read_b64_tr_b16 a[104:105], v191 offset:55296\n"
"ds_read_b64_tr_b16 a[106:107], v191 offset:55808\n"
"buffer_load_dwordx4 v194, s[20:23], s78 offen offset:0 lds\n"
"s_waitcnt vmcnt(11)\n"
"s_waitcnt lgkmcnt(5)\n"
"v_mfma_f32_32x32x16_bf16 a[240:255], a[96:99], v[104:107], a[240:255]\n"
"ds_read_b64_tr_b16 a[108:109], v190 offset:55296\n"
"ds_read_b64_tr_b16 a[110:111], v190 offset:55808\n"
"ds_read_b64_tr_b16 a[80:81], v189 offset:0\n"
"s_waitcnt lgkmcnt(5)\n"
"v_mfma_f32_32x32x16_bf16 v[16:31], a[100:103], v[104:107], v[16:31]\n"
"ds_read_b64_tr_b16 a[82:83], v189 offset:512\n"
"buffer_load_dwordx4 v195, s[20:23], s78 offen offset:1024 lds\n"
"s_waitcnt vmcnt(11)\n"
"s_waitcnt lgkmcnt(4)\n"
"v_mfma_f32_32x32x16_bf16 a[240:255], a[104:107], v[108:111], a[240:255]\n"
"ds_read_b64_tr_b16 a[84:85], v188 offset:0\n"
"ds_read_b64_tr_b16 a[86:87], v188 offset:512\n"
"ds_write_b128 v242, v[64:67] offset:32768\n"
"s_waitcnt lgkmcnt(5)\n"
"v_mfma_f32_32x32x16_bf16 v[16:31], a[108:111], v[108:111], v[16:31]\n"
"ds_read_b64_tr_b16 a[88:89], v189 offset:2048\n"
"ds_read_b64_tr_b16 a[90:91], v189 offset:2560\n"
"buffer_load_dwordx4 v196, s[20:23], s78 offen offset:2048 lds\n"
"s_waitcnt lgkmcnt(2)\n"
"s_barrier\n"
"s_waitcnt vmcnt(11)\n"
"v_mfma_f32_32x32x16_bf16 a[240:255], a[80:83], v[112:115], a[240:255]\n"
"ds_read_b64_tr_b16 a[92:93], v188 offset:2048\n"
"ds_read_b64_tr_b16 a[94:95], v188 offset:2560\n"
"buffer_load_dwordx4 v197, s[20:23], s78 offen offset:3072 lds\n"
"v_mfma_f32_32x32x16_bf16 v[16:31], a[84:87], v[112:115], v[16:31]\n"
"ds_read_b64_tr_b16 a[96:97], v189 offset:4096\n"
"ds_read_b64_tr_b16 a[98:99], v189 offset:4608\n"
"s_add_u32 s78, s35, 1433600\n"
"s_add_u32 m0, s85, 131072\n"
"ds_read_b64_tr_b16 a[100:101], v188 offset:4096\n"
"s_waitcnt vmcnt(11)\n"
"s_waitcnt lgkmcnt(5)\n"
"v_mfma_f32_32x32x16_bf16 a[240:255], a[88:91], v[116:119], a[240:255]\n"
"ds_read_b64_tr_b16 a[102:103], v188 offset:4608\n"
"buffer_load_dwordx4 v[148:151], v227, s[24:27], s92 offen offset:1024 sc1\n"
"ds_read_b64_tr_b16 a[104:105], v189 offset:6144\n"
"s_waitcnt lgkmcnt(5)\n"
"v_mfma_f32_32x32x16_bf16 v[16:31], a[92:95], v[116:119], v[16:31]\n"
"ds_read_b64_tr_b16 a[106:107], v189 offset:6656\n"
"ds_write_b128 v242, v[68:71] offset:33792\n"
"s_waitcnt vmcnt(11)\n"
"s_waitcnt lgkmcnt(5)\n"
"v_mfma_f32_32x32x16_bf16 a[240:255], a[96:99], v[120:123], a[240:255]\n"
"ds_read_b64_tr_b16 a[108:109], v188 offset:6144\n"
"ds_read_b64_tr_b16 a[110:111], v188 offset:6656\n"
"buffer_load_dwordx4 v[152:155], v227, s[24:27], s92 offen offset:2048 sc1\n"
"s_waitcnt lgkmcnt(5)\n"
"v_mfma_f32_32x32x16_bf16 v[16:31], a[100:103], v[120:123], v[16:31]\n"
"ds_read_b64_tr_b16 a[80:81], v189 offset:16384\n"
"ds_read_b64_tr_b16 a[82:83], v189 offset:16896\n"
"ds_write_b128 v242, v[72:75] offset:34816\n"
"ds_write_b128 v242, v[76:79] offset:35840\n"
"s_waitcnt vmcnt(11)\n"
"s_waitcnt lgkmcnt(7)\n"
"v_mfma_f32_32x32x16_bf16 a[240:255], a[104:107], v[124:127], a[240:255]\n"
"ds_read_b64_tr_b16 a[84:85], v188 offset:16384\n"
"ds_read_b64_tr_b16 a[86:87], v188 offset:16896\n"
"buffer_load_dwordx4 v[156:159], v227, s[24:27], s92 offen offset:3072 sc1\n"
"s_waitcnt lgkmcnt(6)\n"
"v_mfma_f32_32x32x16_bf16 v[16:31], a[108:111], v[124:127], v[16:31]\n"
"ds_read_b64_tr_b16 a[88:89], v189 offset:18432\n"
"ds_read_b64_tr_b16 a[90:91], v189 offset:18944\n"
"buffer_load_dwordx4 v[32:35], v227, s[24:27], s93 offen offset:0 sc1\n"
"s_waitcnt lgkmcnt(4)\n"
"s_barrier\n"
"s_waitcnt vmcnt(12)\n"
"v_mfma_f32_32x32x16_bf16 a[240:255], a[80:83], v[128:131], a[240:255]\n"
"ds_read_b64_tr_b16 a[92:93], v188 offset:18432\n"
"ds_read_b64_tr_b16 a[94:95], v188 offset:18944\n"
"ds_read_b64_tr_b16 a[96:97], v189 offset:20480\n"
"s_waitcnt lgkmcnt(5)\n"
"v_mfma_f32_32x32x16_bf16 v[16:31], a[84:87], v[128:131], v[16:31]\n"
"ds_read_b64_tr_b16 a[98:99], v189 offset:20992\n"
"buffer_load_dwordx4 v194, s[20:23], s78 offen offset:0 lds\n"
"s_waitcnt vmcnt(12)\n"
"s_waitcnt lgkmcnt(4)\n"
"v_mfma_f32_32x32x16_bf16 a[240:255], a[88:91], v[132:135], a[240:255]\n"
"ds_read_b64_tr_b16 a[100:101], v188 offset:20480\n"
"ds_read_b64_tr_b16 a[102:103], v188 offset:20992\n"
"ds_read_b64_tr_b16 a[104:105], v189 offset:22528\n"
"s_waitcnt lgkmcnt(5)\n"
"v_mfma_f32_32x32x16_bf16 v[16:31], a[92:95], v[132:135], v[16:31]\n"
"ds_read_b64_tr_b16 a[106:107], v189 offset:23040\n"
"buffer_load_dwordx4 v195, s[20:23], s78 offen offset:1024 lds\n"
"s_waitcnt vmcnt(12)\n"
"s_waitcnt lgkmcnt(4)\n"
"v_mfma_f32_32x32x16_bf16 a[240:255], a[96:99], v[136:139], a[240:255]\n"
"ds_read_b64_tr_b16 a[108:109], v188 offset:22528\n"
"ds_read_b64_tr_b16 a[110:111], v188 offset:23040\n"
"ds_read_b64_tr_b16 a[80:81], v189 offset:32768\n"
"s_waitcnt lgkmcnt(5)\n"
"v_mfma_f32_32x32x16_bf16 v[16:31], a[100:103], v[136:139], v[16:31]\n"
"ds_read_b64_tr_b16 a[82:83], v189 offset:33280\n"
"buffer_load_dwordx4 v196, s[20:23], s78 offen offset:2048 lds\n"
"s_waitcnt vmcnt(12)\n"
"s_waitcnt lgkmcnt(4)\n"
"v_mfma_f32_32x32x16_bf16 a[240:255], a[104:107], v[140:143], a[240:255]\n"
"ds_read_b64_tr_b16 a[84:85], v188 offset:32768\n"
"ds_read_b64_tr_b16 a[86:87], v188 offset:33280\n"
"buffer_load_dwordx4 v197, s[20:23], s78 offen offset:3072 lds\n"
"s_waitcnt lgkmcnt(4)\n"
"v_mfma_f32_32x32x16_bf16 v[16:31], a[108:111], v[140:143], v[16:31]\n"
"ds_read_b64_tr_b16 a[88:89], v189 offset:34816\n"
"ds_read_b64_tr_b16 a[90:91], v189 offset:35328\n"
"ds_read_b64_tr_b16 a[92:93], v188 offset:34816\n"
"s_waitcnt vmcnt(8)\n"
"s_barrier\n"
"s_waitcnt lgkmcnt(5)\n"
"v_mfma_f32_32x32x16_bf16 a[240:255], a[80:83], v[144:147], a[240:255]\n"
"ds_read_b64_tr_b16 a[94:95], v188 offset:35328\n"
"buffer_load_dwordx4 v[36:39], v227, s[24:27], s93 offen offset:1024 sc1\n"
"ds_read_b64_tr_b16 a[96:97], v189 offset:36864\n"
"s_waitcnt lgkmcnt(5)\n"
"v_mfma_f32_32x32x16_bf16 v[16:31], a[84:87], v[144:147], v[16:31]\n"
"ds_read_b64_tr_b16 a[98:99], v189 offset:37376\n"
"ds_read_b64_tr_b16 a[100:101], v188 offset:36864\n"
"ds_read_b64_tr_b16 a[102:103], v188 offset:37376\n"
"v_lshlrev_b32 v64, 16, v4\n"
"v_and_b32 v65, 0xffff0000, v4\n"
"s_waitcnt vmcnt(8)\n"
"s_waitcnt lgkmcnt(6)\n"
"v_mfma_f32_32x32x16_bf16 a[240:255], a[88:91], v[148:151], a[240:255]\n"
"buffer_load_dwordx4 v[40:43], v227, s[24:27], s93 offen offset:2048 sc1\n"
"ds_read_b64_tr_b16 a[104:105], v189 offset:38912\n"
"ds_read_b64_tr_b16 a[106:107], v189 offset:39424\n"
"s_waitcnt lgkmcnt(6)\n"
"v_mfma_f32_32x32x16_bf16 v[16:31], a[92:95], v[148:151], v[16:31]\n"
"ds_read_b64_tr_b16 a[108:109], v188 offset:38912\n"
"ds_read_b64_tr_b16 a[110:111], v188 offset:39424\n"
"ds_read_b64_tr_b16 a[80:81], v189 offset:49152\n"
"v_lshlrev_b32 v66, 16, v5\n"
"v_and_b32 v67, 0xffff0000, v5\n"
"s_waitcnt vmcnt(8)\n"
"s_waitcnt lgkmcnt(5)\n"
"v_mfma_f32_32x32x16_bf16 a[240:255], a[96:99], v[152:155], a[240:255]\n"
"buffer_load_dwordx4 v[44:47], v227, s[24:27], s93 offen offset:3072 sc1\n"
"ds_read_b64_tr_b16 a[82:83], v189 offset:49664\n"
"ds_read_b64_tr_b16 a[84:85], v188 offset:49152\n"
"ds_read_b64_tr_b16 a[86:87], v188 offset:49664\n"
"v_mfma_f32_32x32x16_bf16 v[16:31], a[100:103], v[152:155], v[16:31]\n"
"buffer_load_dwordx4 v[48:51], v227, s[24:27], s94 offen offset:0 sc1\n"
"ds_read_b64_tr_b16 a[88:89], v189 offset:51200\n"
"ds_read_b64_tr_b16 a[90:91], v189 offset:51712\n"
"s_waitcnt vmcnt(9)\n"
"s_waitcnt lgkmcnt(6)\n"
"v_mfma_f32_32x32x16_bf16 a[240:255], a[104:107], v[156:159], a[240:255]\n"
"ds_read_b64_tr_b16 a[92:93], v188 offset:51200\n"
"ds_read_b64_tr_b16 a[94:95], v188 offset:51712\n"
"ds_read_b64_tr_b16 a[96:97], v189 offset:53248\n"
"v_lshlrev_b32 v68, 16, v6\n"
"v_and_b32 v69, 0xffff0000, v6\n"
"v_lshlrev_b32 v70, 16, v7\n"
"v_mfma_f32_32x32x16_bf16 v[16:31], a[108:111], v[156:159], v[16:31]\n"
"buffer_load_dwordx4 v[52:55], v227, s[24:27], s94 offen offset:1024 sc1\n"
"ds_read_b64_tr_b16 a[98:99], v189 offset:53760\n"
"ds_read_b64_tr_b16 a[100:101], v188 offset:53248\n"
"s_waitcnt vmcnt(5)\n"
"s_barrier\n"
"s_waitcnt lgkmcnt(5)\n"
"v_mfma_f32_32x32x16_bf16 a[240:255], a[80:83], v[32:35], a[240:255]\n"
"ds_read_b64_tr_b16 a[102:103], v188 offset:53760\n"
"ds_read_b64_tr_b16 a[104:105], v189 offset:55296\n"
"ds_read_b64_tr_b16 a[106:107], v189 offset:55808\n"
"v_and_b32 v71, 0xffff0000, v7\n"
"v_lshlrev_b32 v72, 16, v8\n"
"v_and_b32 v73, 0xffff0000, v8\n"
"v_mfma_f32_32x32x16_bf16 v[16:31], a[84:87], v[32:35], v[16:31]\n"
"buffer_load_dwordx4 v[56:59], v227, s[24:27], s94 offen offset:2048 sc1\n"
"ds_read_b64_tr_b16 a[108:109], v188 offset:55296\n"
"ds_read_b64_tr_b16 a[110:111], v188 offset:55808\n"
"ds_read_b64_tr_b16 a[80:81], v193 offset:0\n"
"s_waitcnt vmcnt(5)\n"
"v_mfma_f32_32x32x16_bf16 a[240:255], a[88:91], v[36:39], a[240:255]\n"
"ds_read_b64_tr_b16 a[82:83], v193 offset:512\n"
"ds_read_b64_tr_b16 a[84:85], v193 offset:2048\n"
"ds_read_b64_tr_b16 a[86:87], v193 offset:2560\n"
"v_lshlrev_b32 v74, 16, v9\n"
"v_and_b32 v75, 0xffff0000, v9\n"
"s_waitcnt lgkmcnt(6)\n"
"v_mfma_f32_32x32x16_bf16 v[16:31], a[92:95], v[36:39], v[16:31]\n"
"buffer_load_dwordx4 v[60:63], v227, s[24:27], s94 offen offset:3072 sc1\n"
"ds_read_b64_tr_b16 a[88:89], v193 offset:4096\n"
"ds_read_b64_tr_b16 a[90:91], v193 offset:4608\n"
"v_lshlrev_b32 v76, 16, v10\n"
"s_waitcnt vmcnt(5)\n"
"v_mfma_f32_32x32x16_bf16 a[240:255], a[96:99], v[40:43], a[240:255]\n"
"ds_read_b64_tr_b16 a[92:93], v193 offset:6144\n"
"ds_read_b64_tr_b16 a[94:95], v193 offset:6656\n"
"v_and_b32 v77, 0xffff0000, v10\n"
"v_lshlrev_b32 v78, 16, v11\n"
"v_and_b32 v79, 0xffff0000, v11\n"
"v_mov_b32 v221, v220\n"
"v_mfma_f32_32x32x16_bf16 v[16:31], a[100:103], v[40:43], v[16:31]\n"
"ds_read_b64_tr_b16 a[96:97], v192 offset:0\n"
"ds_read_b64_tr_b16 a[98:99], v192 offset:512\n"
"v_and_b32 v202, 15, v1\n"
"v_mul_u32_u24 v202, 896, v202\n"
"v_lshrrev_b32 v203, 5, v1\n"
"v_lshl_add_u32 v202, v203, 4, v202\n"
"s_waitcnt vmcnt(4)\n"
"v_mfma_f32_32x32x16_bf16 a[240:255], a[104:107], v[44:47], a[240:255]\n"
"ds_read_b64_tr_b16 a[100:101], v192 offset:2048\n"
"ds_read_b64_tr_b16 a[102:103], v192 offset:2560\n"
"v_and_b32 v203, 16, v1\n"
"v_lshl_add_u32 v202, v203, 1, v202\n"
"s_mul_i32 s51, s81, 28672\n"
"s_waitcnt lgkmcnt(4)\n"
"v_mfma_f32_32x32x16_bf16 v[16:31], a[108:111], v[44:47], v[16:31]\n"
"ds_read_b64_tr_b16 a[104:105], v192 offset:4096\n"
"ds_read_b64_tr_b16 a[106:107], v192 offset:4608\n"
"s_lshl_b32 s76, s72, 7\n"
"s_add_u32 s51, s51, s76\n"
"s_lshl_b32 s76, s82, 6\n"
"s_add_u32 s51, s51, s76\n"
"s_barrier\n"
"s_waitcnt vmcnt(3)\n"
"v_mfma_f32_32x32x16_bf16 a[240:255], a[80:83], v[48:51], a[240:255]\n"
"ds_read_b64_tr_b16 a[108:109], v192 offset:6144\n"
"ds_read_b64_tr_b16 a[110:111], v192 offset:6656\n"
"s_add_u32 s50, s51, 14336\n"
"s_waitcnt vmcnt(2)\n"
"v_mfma_f32_32x32x16_bf16 a[240:255], a[84:87], v[52:55], a[240:255]\n"
"s_waitcnt vmcnt(1)\n"
"v_mfma_f32_32x32x16_bf16 a[240:255], a[88:91], v[56:59], a[240:255]\n"
"s_waitcnt vmcnt(0)\n"
"v_mfma_f32_32x32x16_bf16 a[240:255], a[92:95], v[60:63], a[240:255]\n"
"s_waitcnt lgkmcnt(0)\n"
"v_mfma_f32_32x32x16_bf16 v[16:31], a[96:99], v[48:51], v[16:31]\n"
"v_mfma_f32_32x32x16_bf16 v[16:31], a[100:103], v[52:55], v[16:31]\n"
"v_mfma_f32_32x32x16_bf16 v[16:31], a[104:107], v[56:59], v[16:31]\n"
"s_nop 7\n"
"ds_write_b128 v218, a[240:243] offset:0\n"
"ds_write_b128 v218, a[244:247] offset:1024\n"
"v_mfma_f32_32x32x16_bf16 v[16:31], a[108:111], v[60:63], v[16:31]\n"
"ds_write_b128 v218, a[248:251] offset:2048\n"
"ds_write_b128 v218, a[252:255] offset:3072\n"
"s_nop 9\n"
"v_cmp_u_f32 vcc, v16, v16\n"
"s_nop 0\n"
"s_cbranch_vccnz L_redo_44_%=\n"
"L_redone_45_%=:\n"
"s_waitcnt lgkmcnt(0)\n"
"s_barrier\n"
"ds_read_b128 v[80:83], v219 offset:0\n"
"ds_read_b128 v[84:87], v219 offset:1024\n"
"ds_read_b128 v[88:91], v219 offset:2048\n"
"ds_read_b128 v[92:95], v219 offset:3072\n"
"s_waitcnt lgkmcnt(3)\n"
"v_pk_add_f32 v[16:17], v[16:17], v[80:81]\n"
"v_pk_fma_f32 v[16:17], v[16:17], v[220:221], v[64:65]\n"
"v_cvt_pk_bf16_f32 v206, v16, v17\n"
"v_pk_add_f32 v[18:19], v[18:19], v[82:83]\n"
"v_pk_fma_f32 v[18:19], v[18:19], v[220:221], v[66:67]\n"
"v_cvt_pk_bf16_f32 v207, v18, v19\n"
"s_waitcnt lgkmcnt(2)\n"
"v_pk_add_f32 v[20:21], v[20:21], v[84:85]\n"
"v_pk_fma_f32 v[20:21], v[20:21], v[220:221], v[68:69]\n"
"v_cvt_pk_bf16_f32 v208, v20, v21\n"
"v_pk_add_f32 v[22:23], v[22:23], v[86:87]\n"
"v_pk_fma_f32 v[22:23], v[22:23], v[220:221], v[70:71]\n"
"v_cvt_pk_bf16_f32 v209, v22, v23\n"
"s_waitcnt lgkmcnt(1)\n"
"v_pk_add_f32 v[24:25], v[24:25], v[88:89]\n"
"v_pk_fma_f32 v[24:25], v[24:25], v[220:221], v[72:73]\n"
"v_cvt_pk_bf16_f32 v210, v24, v25\n"
"v_pk_add_f32 v[26:27], v[26:27], v[90:91]\n"
"v_pk_fma_f32 v[26:27], v[26:27], v[220:221], v[74:75]\n"
"v_cvt_pk_bf16_f32 v211, v26, v27\n"
"s_waitcnt lgkmcnt(0)\n"
"v_pk_add_f32 v[28:29], v[28:29], v[92:93]\n"
"v_pk_fma_f32 v[28:29], v[28:29], v[220:221], v[76:77]\n"
"v_cvt_pk_bf16_f32 v212, v28, v29\n"
"v_pk_add_f32 v[30:31], v[30:31], v[94:95]\n"
"v_pk_fma_f32 v[30:31], v[30:31], v[220:221], v[78:79]\n"
"v_cvt_pk_bf16_f32 v213, v30, v31\n"
"v_permlane16_swap_b32 v206, v210\n"
"v_permlane16_swap_b32 v207, v211\n"
"v_permlane16_swap_b32 v208, v212\n"
"v_permlane16_swap_b32 v209, v213\n"
"buffer_store_dwordx4 v[206:209], v202, s[28:31], s51 offen\n"
"buffer_store_dwordx4 v[210:213], v202, s[28:31], s50 offen\n"
"s_or_b32 s75, s87, s68\n"
"s_cmp_lg_u32 s75, 0\n"
"s_cbranch_scc1 L_nn_46_%=\n"
"s_mov_b64 exec, 1\n"
"buffer_store_dword v225, off, s[24:27], s75 sc1\n"
"s_mov_b64 exec, -1\n"
"L_nn_46_%=:\n"
"s_waitcnt vmcnt(0) lgkmcnt(0)\n"
"s_endpgm\n"
"L_helper_20_%=:\n"
"s_mul_i32 s42, s80, 114688\n"
"s_mul_i32 s76, s71, 28672\n"
"s_add_u32 s42, s42, s76\n"
"s_add_u32 s42, s42, 6348800\n"
"s_lshl_b32 s43, s80, 3\n"
"s_lshl_b32 s76, s71, 2\n"
"s_add_u32 s43, s43, s76\n"
"s_add_u32 s43, s43, 12288\n"
"buffer_store_dwordx4 a[204:207], v227, s[24:27], s42 offen offset:0 sc1\n"
"buffer_store_dwordx4 a[204:207], v227, s[24:27], s42 offen offset:1024 sc1\n"
"buffer_store_dwordx4 a[204:207], v227, s[24:27], s42 offen offset:2048 sc1\n"
"buffer_store_dwordx4 a[204:207], v227, s[24:27], s42 offen offset:3072 sc1\n"
"s_add_u32 s42, s42, 4096\n"
"buffer_store_dwordx4 a[204:207], v227, s[24:27], s42 offen offset:0 sc1\n"
"buffer_store_dwordx4 a[204:207], v227, s[24:27], s42 offen offset:1024 sc1\n"
"buffer_store_dwordx4 a[204:207], v227, s[24:27], s42 offen offset:2048 sc1\n"
"buffer_store_dwordx4 a[204:207], v227, s[24:27], s42 offen offset:3072 sc1\n"
"s_mov_b32 s78, 458752\n"
"s_add_u32 m0, s85, 81920\n"
"s_nop 0\n"
"buffer_load_dwordx4 v12, s[16:19], s78 offen offset:0 lds\n"
"buffer_load_dwordx4 v13, s[16:19], s78 offen offset:1024 lds\n"
"buffer_load_dwordx4 v14, s[16:19], s78 offen offset:2048 lds\n"
"buffer_load_dwordx4 v15, s[16:19], s78 offen offset:3072 lds\n"
"s_mov_b32 s78, 688128\n"
"s_add_u32 m0, s85, 98304\n"
"s_nop 0\n"
"buffer_load_dwordx4 v12, s[16:19], s78 offen offset:0 lds\n"
"buffer_load_dwordx4 v13, s[16:19], s78 offen offset:1024 lds\n"
"buffer_load_dwordx4 v14, s[16:19], s78 offen offset:2048 lds\n"
"buffer_load_dwordx4 v15, s[16:19], s78 offen offset:3072 lds\n"
"s_mov_b32 s78, 917504\n"
"s_add_u32 m0, s85, 114688\n"
"s_nop 0\n"
"buffer_load_dwordx4 v12, s[16:19], s78 offen offset:0 lds\n"
"buffer_load_dwordx4 v13, s[16:19], s78 offen offset:1024 lds\n"
"buffer_load_dwordx4 v14, s[16:19], s78 offen offset:2048 lds\n"
"buffer_load_dwordx4 v15, s[16:19], s78 offen offset:3072 lds\n"
"s_mov_b32 s78, 1146880\n"
"s_mov_b32 m0, s85\n"
"s_nop 0\n"
"buffer_load_dwordx4 v12, s[16:19], s78 offen offset:0 lds\n"
"buffer_load_dwordx4 v13, s[16:19], s78 offen offset:1024 lds\n"
"buffer_load_dwordx4 v14, s[16:19], s78 offen offset:2048 lds\n"
"buffer_load_dwordx4 v15, s[16:19], s78 offen offset:3072 lds\n"
"s_mov_b32 s78, 1376256\n"
"s_add_u32 m0, s85, 16384\n"
"s_nop 0\n"
"buffer_load_dwordx4 v12, s[16:19], s78 offen offset:0 lds\n"
"buffer_load_dwordx4 v13, s[16:19], s78 offen offset:1024 lds\n"
"buffer_load_dwordx4 v14, s[16:19], s78 offen offset:2048 lds\n"
"buffer_load_dwordx4 v15, s[16:19], s78 offen offset:3072 lds\n"
"v_mov_b32 v202, s77\n"
"v_mov_b32 v203, s77\n"
"v_mov_b32 v204, s77\n"
"v_mov_b32 v205, s77\n"
"s_cmp_eq_u32 s68, 1\n"
"s_cselect_b64 vcc, -1, 0\n"
"v_cndmask_b32 v176, v176, v177, vcc\n"
"s_cmp_eq_u32 s68, 2\n"
"s_cselect_b64 vcc, -1, 0\n"
"v_cndmask_b32 v176, v176, v178, vcc\n"
"s_cmp_eq_u32 s68, 3\n"
"s_cselect_b64 vcc, -1, 0\n"
"v_cndmask_b32 v176, v176, v179, vcc\n"
"v_add_u32 v177, 65536, v176\n"
"v_add_u32 v178, 131072, v176\n"
"s_add_u32 s42, s42, 4096\n"
"buffer_store_dwordx4 a[204:207], v227, s[24:27], s42 offen offset:0 sc1\n"
"buffer_store_dwordx4 a[204:207], v227, s[24:27], s42 offen offset:1024 sc1\n"
"buffer_store_dwordx4 a[204:207], v227, s[24:27], s42 offen offset:2048 sc1\n"
"buffer_store_dwordx4 a[204:207], v227, s[24:27], s42 offen offset:3072 sc1\n"
"s_add_u32 s42, s42, 4096\n"
"buffer_store_dwordx4 a[204:207], v227, s[24:27], s42 offen offset:0 sc1\n"
"buffer_store_dwordx4 a[204:207], v227, s[24:27], s42 offen offset:1024 sc1\n"
"buffer_store_dwordx4 a[204:207], v227, s[24:27], s42 offen offset:2048 sc1\n"
"buffer_store_dwordx4 a[204:207], v227, s[24:27], s42 offen offset:3072 sc1\n"
"s_waitcnt vmcnt(8)\n"
"s_barrier\n"
"ds_read_b64_tr_b16 v[80:81], v178 offset:0\n"
"ds_read_b64_tr_b16 v[82:83], v178 offset:512\n"
"ds_read_b64_tr_b16 v[84:85], v178 offset:2048\n"
"ds_read_b64_tr_b16 v[86:87], v178 offset:2560\n"
"ds_read_b64_tr_b16 v[88:89], v178 offset:4096\n"
"ds_read_b64_tr_b16 v[90:91], v178 offset:4608\n"
"ds_read_b64_tr_b16 v[92:93], v178 offset:6144\n"
"ds_read_b64_tr_b16 v[94:95], v178 offset:6656\n"
"s_add_u32 s42, s42, 4096\n"
"buffer_store_dwordx4 a[204:207], v227, s[24:27], s42 offen offset:0 sc1\n"
"ds_read_b64_tr_b16 v[96:97], v177 offset:0\n"
"ds_read_b64_tr_b16 v[98:99], v177 offset:512\n"
"ds_read_b64_tr_b16 v[100:101], v177 offset:2048\n"
"ds_read_b64_tr_b16 v[102:103], v177 offset:2560\n"
"ds_read_b64_tr_b16 v[104:105], v177 offset:4096\n"
"ds_read_b64_tr_b16 v[106:107], v177 offset:4608\n"
"ds_read_b64_tr_b16 v[108:109], v177 offset:6144\n"
"s_waitcnt lgkmcnt(14)\n"
"ds_read_b64_tr_b16 v[110:111], v177 offset:6656\n"
"buffer_store_dwordx4 a[204:207], v227, s[24:27], s42 offen offset:1024 sc1\n"
"s_waitcnt lgkmcnt(14)\n"
"v_mfma_f32_32x32x16_bf16 v[16:31], v[202:205], v[80:83], 0\n"
"s_waitcnt lgkmcnt(12)\n"
"v_mfma_f32_32x32x16_bf16 v[16:31], v[202:205], v[84:87], v[16:31]\n"
"s_waitcnt lgkmcnt(10)\n"
"v_mfma_f32_32x32x16_bf16 v[16:31], v[202:205], v[88:91], v[16:31]\n"
"s_waitcnt lgkmcnt(8)\n"
"v_mfma_f32_32x32x16_bf16 v[16:31], v[202:205], v[92:95], v[16:31]\n"
"ds_read_b64_tr_b16 v[80:81], v177 offset:16384\n"
"ds_read_b64_tr_b16 v[82:83], v177 offset:16896\n"
"ds_read_b64_tr_b16 v[84:85], v177 offset:18432\n"
"ds_read_b64_tr_b16 v[86:87], v177 offset:18944\n"
"ds_read_b64_tr_b16 v[88:89], v177 offset:20480\n"
"ds_read_b64_tr_b16 v[90:91], v177 offset:20992\n"
"ds_read_b64_tr_b16 v[92:93], v177 offset:22528\n"
"s_waitcnt lgkmcnt(14)\n"
"ds_read_b64_tr_b16 v[94:95], v177 offset:23040\n"
"buffer_store_dwordx4 a[204:207], v227, s[24:27], s42 offen offset:2048 sc1\n"
"s_waitcnt lgkmcnt(14)\n"
"v_mfma_f32_32x32x16_bf16 v[16:31], v[202:205], v[96:99], v[16:31]\n"
"s_waitcnt lgkmcnt(12)\n"
"v_mfma_f32_32x32x16_bf16 v[16:31], v[202:205], v[100:103], v[16:31]\n"
"s_waitcnt lgkmcnt(10)\n"
"v_mfma_f32_32x32x16_bf16 v[16:31], v[202:205], v[104:107], v[16:31]\n"
"s_waitcnt lgkmcnt(8)\n"
"v_mfma_f32_32x32x16_bf16 v[16:31], v[202:205], v[108:111], v[16:31]\n"
"ds_read_b64_tr_b16 v[96:97], v177 offset:32768\n"
"ds_read_b64_tr_b16 v[98:99], v177 offset:33280\n"
"ds_read_b64_tr_b16 v[100:101], v177 offset:34816\n"
"ds_read_b64_tr_b16 v[102:103], v177 offset:35328\n"
"ds_read_b64_tr_b16 v[104:105], v177 offset:36864\n"
"ds_read_b64_tr_b16 v[106:107], v177 offset:37376\n"
"ds_read_b64_tr_b16 v[108:109], v177 offset:38912\n"
"s_waitcnt lgkmcnt(14)\n"
"ds_read_b64_tr_b16 v[110:111], v177 offset:39424\n"
"buffer_store_dwordx4 a[204:207], v227, s[24:27], s42 offen offset:3072 sc1\n"
"s_waitcnt lgkmcnt(14)\n"
"v_mfma_f32_32x32x16_bf16 v[16:31], v[202:205], v[80:83], v[16:31]\n"
"s_waitcnt lgkmcnt(12)\n"
"v_mfma_f32_32x32x16_bf16 v[16:31], v[202:205], v[84:87], v[16:31]\n"
"s_waitcnt lgkmcnt(10)\n"
"v_mfma_f32_32x32x16_bf16 v[16:31], v[202:205], v[88:91], v[16:31]\n"
"s_waitcnt lgkmcnt(8)\n"
"v_mfma_f32_32x32x16_bf16 v[16:31], v[202:205], v[92:95], v[16:31]\n"
"ds_read_b64_tr_b16 v[80:81], v177 offset:49152\n"
"ds_read_b64_tr_b16 v[82:83], v177 offset:49664\n"
"ds_read_b64_tr_b16 v[84:85], v177 offset:51200\n"
"ds_read_b64_tr_b16 v[86:87], v177 offset:51712\n"
"ds_read_b64_tr_b16 v[88:89], v177 offset:53248\n"
"ds_read_b64_tr_b16 v[90:91], v177 offset:53760\n"
"ds_read_b64_tr_b16 v[92:93], v177 offset:55296\n"
"s_waitcnt lgkmcnt(14)\n"
"ds_read_b64_tr_b16 v[94:95], v177 offset:55808\n"
"s_add_u32 s42, s42, 4096\n"
"buffer_store_dwordx4 a[204:207], v227, s[24:27], s42 offen offset:0 sc1\n"
"s_waitcnt lgkmcnt(14)\n"
"v_mfma_f32_32x32x16_bf16 v[16:31], v[202:205], v[96:99], v[16:31]\n"
"s_waitcnt lgkmcnt(12)\n"
"v_mfma_f32_32x32x16_bf16 v[16:31], v[202:205], v[100:103], v[16:31]\n"
"s_waitcnt lgkmcnt(10)\n"
"v_mfma_f32_32x32x16_bf16 v[16:31], v[202:205], v[104:107], v[16:31]\n"
"s_waitcnt lgkmcnt(8)\n"
"v_mfma_f32_32x32x16_bf16 v[16:31], v[202:205], v[108:111], v[16:31]\n"
"ds_read_b64_tr_b16 v[96:97], v176 offset:0\n"
"ds_read_b64_tr_b16 v[98:99], v176 offset:512\n"
"ds_read_b64_tr_b16 v[100:101], v176 offset:2048\n"
"ds_read_b64_tr_b16 v[102:103], v176 offset:2560\n"
"ds_read_b64_tr_b16 v[104:105], v176 offset:4096\n"
"ds_read_b64_tr_b16 v[106:107], v176 offset:4608\n"
"ds_read_b64_tr_b16 v[108:109], v176 offset:6144\n"
"s_waitcnt lgkmcnt(14)\n"
"ds_read_b64_tr_b16 v[110:111], v176 offset:6656\n"
"buffer_store_dwordx4 a[204:207], v227, s[24:27], s42 offen offset:1024 sc1\n"
"s_waitcnt lgkmcnt(14)\n"
"v_mfma_f32_32x32x16_bf16 v[16:31], v[202:205], v[80:83], v[16:31]\n"
"s_waitcnt lgkmcnt(12)\n"
"v_mfma_f32_32x32x16_bf16 v[16:31], v[202:205], v[84:87], v[16:31]\n"
"s_waitcnt lgkmcnt(10)\n"
"v_mfma_f32_32x32x16_bf16 v[16:31], v[202:205], v[88:91], v[16:31]\n"
"s_waitcnt lgkmcnt(8)\n"
"v_mfma_f32_32x32x16_bf16 v[16:31], v[202:205], v[92:95], v[16:31]\n"
"ds_read_b64_tr_b16 v[80:81], v176 offset:16384\n"
"ds_read_b64_tr_b16 v[82:83], v176 offset:16896\n"
"ds_read_b64_tr_b16 v[84:85], v176 offset:18432\n"
"ds_read_b64_tr_b16 v[86:87], v176 offset:18944\n"
"ds_read_b64_tr_b16 v[88:89], v176 offset:20480\n"
"ds_read_b64_tr_b16 v[90:91], v176 offset:20992\n"
"ds_read_b64_tr_b16 v[92:93], v176 offset:22528\n"
"s_waitcnt lgkmcnt(14)\n"
"ds_read_b64_tr_b16 v[94:95], v176 offset:23040\n"
"buffer_store_dwordx4 a[204:207], v227, s[24:27], s42 offen offset:2048 sc1\n"
"s_waitcnt lgkmcnt(14)\n"
"v_mfma_f32_32x32x16_bf16 v[16:31], v[202:205], v[96:99], v[16:31]\n"
"s_waitcnt lgkmcnt(12)\n"
"v_mfma_f32_32x32x16_bf16 v[16:31], v[202:205], v[100:103], v[16:31]\n"
"s_waitcnt lgkmcnt(10)\n"
"v_mfma_f32_32x32x16_bf16 v[16:31], v[202:205], v[104:107], v[16:31]\n"
"s_waitcnt lgkmcnt(8)\n"
"v_mfma_f32_32x32x16_bf16 v[16:31], v[202:205], v[108:111], v[16:31]\n"
"s_waitcnt lgkmcnt(6)\n"
"v_mfma_f32_32x32x16_bf16 v[16:31], v[202:205], v[80:83], v[16:31]\n"
"s_waitcnt lgkmcnt(4)\n"
"v_mfma_f32_32x32x16_bf16 v[16:31], v[202:205], v[84:87], v[16:31]\n"
"s_waitcnt lgkmcnt(2)\n"
"v_mfma_f32_32x32x16_bf16 v[16:31], v[202:205], v[88:91], v[16:31]\n"
"s_waitcnt lgkmcnt(0)\n"
"v_mfma_f32_32x32x16_bf16 v[16:31], v[202:205], v[92:95], v[16:31]\n"
"s_lshl_b32 s75, s86, 9\n"
"s_lshl_b32 s76, s68, 2\n"
"s_add_u32 s75, s75, s76\n"
"s_add_u32 s75, s75, 4947968\n"
"s_lshl_b32 s76, s86, 7\n"
"s_lshl_b32 s74, s68, 2\n"
"s_add_u32 s76, s76, s74\n"
"s_add_u32 s76, s76, 16384\n"
"s_cmp_ge_u32 s69, 14\n"
"s_cselect_b32 s75, 2130706432, s75\n"
"s_cselect_b32 s76, 2130706432, s76\n"
"s_nop 0\n"
"v_cvt_pk_bf16_f32 v207, v16, v16\n"
"v_lshlrev_b32 v207, 16, v207\n"
"v_sub_f32 v206, v16, v207\n"
"v_cvt_pk_bf16_f32 v206, v207, v206\n"
"s_mov_b64 exec, s[52:53]\n"
"buffer_store_dword v206, v227, s[24:27], s75 offen sc1\n"
"s_mov_b64 exec, -1\n"
"buffer_store_dwordx4 a[204:207], v227, s[24:27], s42 offen offset:3072 sc1\n"
"s_add_u32 s42, s42, 4096\n"
"buffer_store_dwordx4 a[204:207], v227, s[24:27], s42 offen offset:0 sc1\n"
"buffer_store_dwordx4 a[204:207], v227, s[24:27], s42 offen offset:1024 sc1\n"
"buffer_store_dwordx4 a[204:207], v227, s[24:27], s42 offen offset:2048 sc1\n"
"buffer_store_dwordx4 a[204:207], v227, s[24:27], s42 offen offset:3072 sc1\n"
"s_waitcnt vmcnt(5)\n"
"s_mov_b64 exec, 1\n"
"buffer_store_dword v225, off, s[24:27], s76 sc1\n"
"s_mov_b64 exec, -1\n"
"v_lshrrev_b32 v16, 3, v1\n"
"v_and_b32 v17, 7, v1\n"
"v_mul_u32_u24 v18, 3584, v16\n"
"v_lshl_add_u32 v18, v17, 8, v18\n"
"v_mul_u32_u24 v19, 896, v16\n"
"v_lshl_add_u32 v19, v17, 7, v19\n"
"s_and_b32 s73, s69, 3\n"
"s_mul_i32 s74, s73, 57344\n"
"s_mul_i32 s75, s83, 28672\n"
"s_add_u32 s74, s74, s75\n"
"s_mul_i32 s75, s71, 1792\n"
"s_add_u32 s74, s74, s75\n"
"s_add_u32 s74, s74, 128\n"
"s_lshl_b32 s73, s73, 2\n"
"s_add_u32 s73, s73, s68\n"
"s_lshr_b32 s75, s73, 3\n"
"s_and_b32 s73, s73, 7\n"
"s_mul_i32 s41, s75, 114688\n"
"s_mul_i32 s76, s73, 7168\n"
"s_add_u32 s41, s41, s76\n"
"s_mov_b32 vcc_lo, 0x7f7f7f7f\n"
"s_mov_b32 vcc_hi, 0x7f7f7f7f\n"
"s_waitcnt vmcnt(1)\n"
"s_mov_b64 exec, 1\n"
"buffer_store_dword v225, off, s[24:27], s43 sc1\n"
"s_mov_b64 exec, -1\n"
"s_add_u32 s75, s74, 0\n"
"s_mov_b64 exec, vcc\n"
"buffer_load_dword v20, v18, s[16:19], s75 offen\n"
"s_mov_b64 exec, -1\n"
"s_add_u32 s75, s74, 229376\n"
"s_mov_b64 exec, vcc\n"
"buffer_load_dword v21, v18, s[16:19], s75 offen\n"
"s_mov_b64 exec, -1\n"
"s_add_u32 s75, s74, 458752\n"
"s_mov_b64 exec, vcc\n"
"buffer_load_dword v22, v18, s[16:19], s75 offen\n"
"s_mov_b64 exec, -1\n"
"s_add_u32 s75, s74, 688128\n"
"s_mov_b64 exec, vcc\n"
"buffer_load_dword v23, v18, s[16:19], s75 offen\n"
"s_mov_b64 exec, -1\n"
"s_waitcnt vmcnt(3)\n"
"s_add_u32 s75, s74, 917504\n"
"s_mov_b64 exec, vcc\n"
"buffer_load_dword v24, v18, s[16:19], s75 offen\n"
"s_mov_b64 exec, -1\n"
"s_waitcnt vmcnt(3)\n"
"s_add_u32 s75, s74, 1146880\n"
"s_mov_b64 exec, vcc\n"
"buffer_load_dword v25, v18, s[16:19], s75 offen\n"
"s_mov_b64 exec, -1\n"
"s_waitcnt vmcnt(3)\n"
"s_add_u32 s75, s74, 1376256\n"
"s_mov_b64 exec, vcc\n"
"buffer_load_dword v26, v18, s[16:19], s75 offen\n"
"s_mov_b64 exec, -1\n"
"s_waitcnt vmcnt(3)\n"
"s_add_u32 s75, s41, 0\n"
"s_mov_b64 exec, vcc\n"
"buffer_load_dword v27, v19, s[20:23], s75 offen\n"
"s_mov_b64 exec, -1\n"
"s_waitcnt vmcnt(3)\n"
"s_add_u32 s75, s41, 229376\n"
"s_mov_b64 exec, vcc\n"
"buffer_load_dword v28, v19, s[20:23], s75 offen\n"
"s_mov_b64 exec, -1\n"
"s_waitcnt vmcnt(3)\n"
"s_add_u32 s75, s41, 458752\n"
"s_mov_b64 exec, vcc\n"
"buffer_load_dword v29, v19, s[20:23], s75 offen\n"
"s_mov_b64 exec, -1\n"
"s_waitcnt vmcnt(3)\n"
"s_add_u32 s75, s41, 688128\n"
"s_mov_b64 exec, vcc\n"
"buffer_load_dword v30, v19, s[20:23], s75 offen\n"
"s_mov_b64 exec, -1\n"
"s_waitcnt vmcnt(3)\n"
"s_add_u32 s75, s41, 917504\n"
"s_mov_b64 exec, vcc\n"
"buffer_load_dword v31, v19, s[20:23], s75 offen\n"
"s_mov_b64 exec, -1\n"
"s_waitcnt vmcnt(3)\n"
"s_add_u32 s75, s41, 1146880\n"
"s_mov_b64 exec, vcc\n"
"buffer_load_dword v32, v19, s[20:23], s75 offen\n"
"s_mov_b64 exec, -1\n"
"s_waitcnt vmcnt(3)\n"
"s_add_u32 s75, s41, 1376256\n"
"s_mov_b64 exec, vcc\n"
"buffer_load_dword v33, v19, s[20:23], s75 offen\n"
"s_mov_b64 exec, -1\n"
"s_waitcnt vmcnt(3)\n"
"s_add_u32 s75, s41, 57344\n"
"s_mov_b64 exec, vcc\n"
"buffer_load_dword v34, v19, s[20:23], s75 offen\n"
"s_mov_b64 exec, -1\n"
"s_waitcnt vmcnt(3)\n"
"s_add_u32 s75, s41, 286720\n"
"s_mov_b64 exec, vcc\n"
"buffer_load_dword v35, v19, s[20:23], s75 offen\n"
"s_mov_b64 exec, -1\n"
"s_waitcnt vmcnt(3)\n"
"s_add_u32 s75, s41, 516096\n"
"s_mov_b64 exec, vcc\n"
"buffer_load_dword v36, v19, s[20:23], s75 offen\n"
"s_mov_b64 exec, -1\n"
"s_waitcnt vmcnt(3)\n"
"s_add_u32 s75, s41, 745472\n"
"s_mov_b64 exec, vcc\n"
"buffer_load_dword v37, v19, s[20:23], s75 offen\n"
"s_mov_b64 exec, -1\n"
"s_waitcnt vmcnt(3)\n"
"s_add_u32 s75, s41, 974848\n"
"s_mov_b64 exec, vcc\n"
"buffer_load_dword v38, v19, s[20:23], s75 offen\n"
"s_mov_b64 exec, -1\n"
"s_waitcnt vmcnt(3)\n"
"s_add_u32 s75, s41, 1204224\n"
"s_mov_b64 exec, vcc\n"
"buffer_load_dword v39, v19, s[20:23], s75 offen\n"
"s_mov_b64 exec, -1\n"
"s_waitcnt vmcnt(3)\n"
"s_add_u32 s75, s41, 1433600\n"
"s_mov_b64 exec, vcc\n"
"buffer_load_dword v40, v19, s[20:23], s75 offen\n"
"s_mov_b64 exec, -1\n"
"s_waitcnt vmcnt(3)\n"
"s_waitcnt vmcnt(0) lgkmcnt(0)\n"
"s_endpgm\n"
"s_nop 7\n"
"s_nop 7\n"
"L_res_1_%=:\n"
"ds_read_b128 v[4:7], v120 offset:0\n"
"ds_read_b128 v[8:11], v121 offset:0\n"
"s_branch L_resb_2_%=\n"
"s_nop 7\n"
"s_nop 7\n"
"L_res_3_%=:\n"
"ds_read_b128 v[4:7], v120 offset:16384\n"
"ds_read_b128 v[8:11], v121 offset:16384\n"
"s_branch L_resb_4_%=\n"
"s_nop 7\n"
"s_nop 7\n"
"L_res_5_%=:\n"
"ds_read_b128 v[4:7], v120 offset:32768\n"
"ds_read_b128 v[8:11], v121 offset:32768\n"
"s_branch L_resb_6_%=\n"
"s_nop 7\n"
"s_nop 7\n"
"L_res_7_%=:\n"
"v_add_u32 v120, 65536, v120\n"
"ds_read_b128 v[4:7], v120 offset:0\n"
"v_add_u32 v121, 65536, v121\n"
"ds_read_b128 v[8:11], v121 offset:0\n"
"s_branch L_resb_8_%=\n"
"s_nop 7\n"
"s_nop 7\n"
"L_res_9_%=:\n"
"v_add_u32 v120, 65536, v120\n"
"ds_read_b128 v[4:7], v120 offset:16384\n"
"v_add_u32 v121, 65536, v121\n"
"ds_read_b128 v[8:11], v121 offset:16384\n"
"s_branch L_resb_10_%=\n"
"s_nop 7\n"
"s_nop 7\n"
"L_res_11_%=:\n"
"v_add_u32 v120, 65536, v120\n"
"ds_read_b128 v[4:7], v120 offset:32768\n"
"v_add_u32 v121, 65536, v121\n"
"ds_read_b128 v[8:11], v121 offset:32768\n"
"s_branch L_resb_12_%=\n"
"s_nop 7\n"
"s_nop 7\n"
"L_res_13_%=:\n"
"v_add_u32 v120, 65536, v120\n"
"ds_read_b128 v[4:7], v120 offset:49152\n"
"v_add_u32 v121, 65536, v121\n"
"ds_read_b128 v[8:11], v121 offset:49152\n"
"s_branch L_resb_14_%=\n"
"s_nop 7\n"
"s_nop 7\n"
"L_fs_17_%=:\n"
"s_mov_b32 s79, 20000\n"
"L_fl_47_%=:\n"
"buffer_load_dword v139, v138, s[24:27], 0 offen sc1\n"
"s_waitcnt vmcnt(0)\n"
"v_cmp_ne_u32 vcc, v137, v139\n"
"s_nop 0\n"
"s_cbranch_vccz L_fd_18_%=\n"
"s_sub_u32 s79, s79, 1\n"
"s_cbranch_scc0 L_fl_47_%=\n"
"s_branch L_fd_18_%=\n"
"s_nop 7\n"
"s_nop 7\n"
"L_gretry_19_%=:\n"
"s_sub_u32 s79, s79, 1\n"
"s_cbranch_scc1 L_gdone_16_%=\n"
"buffer_load_dwordx4 v[144:147], v36, s[24:27], s34 offen sc1\n"
"s_add_u32 s41, s34, 4096\n"
"buffer_load_dwordx4 v[148:151], v36, s[24:27], s41 offen sc1\n"
"s_add_u32 s41, s34, 8192\n"
"buffer_load_dwordx4 v[152:155], v36, s[24:27], s41 offen sc1\n"
"s_add_u32 s41, s34, 12288\n"
"buffer_load_dwordx4 v[156:159], v36, s[24:27], s41 offen sc1\n"
"s_add_u32 s41, s34, 16384\n"
"buffer_load_dwordx4 v[160:163], v36, s[24:27], s41 offen sc1\n"
"s_add_u32 s41, s34, 20480\n"
"buffer_load_dwordx4 v[164:167], v36, s[24:27], s41 offen sc1\n"
"s_add_u32 s41, s34, 24576\n"
"buffer_load_dwordx4 v[168:171], v36, s[24:27], s41 offen sc1\n"
"s_add_u32 s41, s34, 28672\n"
"buffer_load_dwordx4 v[172:175], v36, s[24:27], s41 offen sc1\n"
"s_waitcnt vmcnt(0)\n"
"s_branch L_gather_15_%=\n"
"s_nop 7\n"
"s_nop 7\n"
"L_fs_22_%=:\n"
"s_mov_b32 s79, 20000\n"
"L_fl_48_%=:\n"
"buffer_load_dword v226, v224, s[24:27], s51 offen sc1\n"
"s_waitcnt vmcnt(0)\n"
"v_cmp_ne_u32 vcc, v225, v226\n"
"s_nop 0\n"
"s_cbranch_vccz L_fd_23_%=\n"
"s_sub_u32 s79, s79, 1\n"
"s_cbranch_scc0 L_fl_48_%=\n"
"s_branch L_fd_23_%=\n"
"s_nop 7\n"
"s_nop 7\n"
"L_slowo_24_%=:\n"
"s_mov_b32 s79, 20000\n"
"L_sl_49_%=:\n"
"buffer_load_dwordx4 v[80:83], v227, s[24:27], s41 offen offset:0 sc1\n"
"buffer_load_dwordx4 v[84:87], v227, s[24:27], s41 offen offset:1024 sc1\n"
"buffer_load_dwordx4 v[88:91], v227, s[24:27], s41 offen offset:2048 sc1\n"
"buffer_load_dwordx4 v[92:95], v227, s[24:27], s41 offen offset:3072 sc1\n"
"buffer_load_dwordx4 v[96:99], v227, s[24:27], s42 offen offset:0 sc1\n"
"buffer_load_dwordx4 v[100:103], v227, s[24:27], s42 offen offset:1024 sc1\n"
"buffer_load_dwordx4 v[104:107], v227, s[24:27], s42 offen offset:2048 sc1\n"
"buffer_load_dwordx4 v[108:111], v227, s[24:27], s42 offen offset:3072 sc1\n"
"buffer_load_dwordx4 v[112:115], v227, s[24:27], s43 offen offset:0 sc1\n"
"buffer_load_dwordx4 v[116:119], v227, s[24:27], s43 offen offset:1024 sc1\n"
"buffer_load_dwordx4 v[120:123], v227, s[24:27], s43 offen offset:2048 sc1\n"
"buffer_load_dwordx4 v[124:127], v227, s[24:27], s43 offen offset:3072 sc1\n"
"buffer_load_dwordx4 v[128:131], v227, s[24:27], s44 offen offset:0 sc1\n"
"buffer_load_dwordx4 v[132:135], v227, s[24:27], s44 offen offset:1024 sc1\n"
"buffer_load_dwordx4 v[136:139], v227, s[24:27], s44 offen offset:2048 sc1\n"
"buffer_load_dwordx4 v[140:143], v227, s[24:27], s44 offen offset:3072 sc1\n"
"s_waitcnt vmcnt(0)\n"
"v_max3_u32 v222, v80, v81, v82\n"
"v_max3_u32 v222, v222, v83, v84\n"
"v_max3_u32 v222, v222, v85, v86\n"
"v_max3_u32 v222, v222, v87, v88\n"
"v_max3_u32 v222, v222, v89, v90\n"
"v_max3_u32 v222, v222, v91, v92\n"
"v_max3_u32 v222, v222, v93, v94\n"
"v_max3_u32 v222, v222, v95, v96\n"
"v_max3_u32 v222, v222, v97, v98\n"
"v_max3_u32 v222, v222, v99, v100\n"
"v_max3_u32 v222, v222, v101, v102\n"
"v_max3_u32 v222, v222, v103, v104\n"
"v_max3_u32 v222, v222, v105, v106\n"
"v_max3_u32 v222, v222, v107, v108\n"
"v_max3_u32 v222, v222, v109, v110\n"
"v_max3_u32 v222, v222, v111, v112\n"
"v_max3_u32 v222, v222, v113, v114\n"
"v_max3_u32 v222, v222, v115, v116\n"
"v_max3_u32 v222, v222, v117, v118\n"
"v_max3_u32 v222, v222, v119, v120\n"
"v_max3_u32 v222, v222, v121, v122\n"
"v_max3_u32 v222, v222, v123, v124\n"
"v_max3_u32 v222, v222, v125, v126\n"
"v_max3_u32 v222, v222, v127, v128\n"
"v_max3_u32 v222, v222, v129, v130\n"
"v_max3_u32 v222, v222, v131, v132\n"
"v_max3_u32 v222, v222, v133, v134\n"
"v_max3_u32 v222, v222, v135, v136\n"
"v_max3_u32 v222, v222, v137, v138\n"
"v_max3_u32 v222, v222, v139, v140\n"
"v_max3_u32 v222, v222, v141, v142\n"
"v_max_u32 v222, v222, v143\n"
"v_cmp_eq_u32 vcc, -1, v222\n"
"s_nop 0\n"
"s_cbranch_vccz L_sd_50_%=\n"
"s_sub_u32 s79, s79, 1\n"
"s_cbranch_scc0 L_sl_49_%=\n"
"L_sd_50_%=:\n"
"s_nop 7\n"
"s_cmp_eq_u32 s95, 0\n"
"s_cbranch_scc1 L_ret_25_%=\n"
"s_cmp_eq_u32 s95, 1\n"
"s_cbranch_scc1 L_ret_26_%=\n"
"s_cmp_eq_u32 s95, 2\n"
"s_cbranch_scc1 L_ret_27_%=\n"
"s_cmp_eq_u32 s95, 3\n"
"s_cbranch_scc1 L_ret_28_%=\n"
"s_cmp_eq_u32 s95, 4\n"
"s_cbranch_scc1 L_ret_29_%=\n"
"s_cmp_eq_u32 s95, 5\n"
"s_cbranch_scc1 L_ret_30_%=\n"
"s_cmp_eq_u32 s95, 6\n"
"s_cbranch_scc1 L_ret_31_%=\n"
"s_branch L_ret_32_%=\n"
"s_nop 7\n"
"s_nop 7\n"
"L_hslow_36_%=:\n"
"L_hp_51_%=:\n"
"s_sub_u32 s79, s79, 1\n"
"s_cbranch_scc1 L_hgo_35_%=\n"
"buffer_load_dwordx2 v[80:81], v223, s[24:27], s39 offen offset:0 sc1\n"
"buffer_load_dwordx2 v[82:83], v223, s[24:27], s39 offen offset:256 sc1\n"
"buffer_load_dwordx2 v[84:85], v223, s[24:27], s39 offen offset:512 sc1\n"
"buffer_load_dwordx2 v[86:87], v223, s[24:27], s39 offen offset:768 sc1\n"
"buffer_load_dwordx2 v[88:89], v223, s[24:27], s39 offen offset:1024 sc1\n"
"buffer_load_dwordx2 v[90:91], v223, s[24:27], s39 offen offset:1280 sc1\n"
"buffer_load_dwordx2 v[92:93], v223, s[24:27], s39 offen offset:1536 sc1\n"
"s_waitcnt vmcnt(0)\n"
"v_max3_u32 v222, v80, v81, v82\n"
"v_max3_u32 v222, v222, v83, v84\n"
"v_max3_u32 v222, v222, v85, v86\n"
"v_max3_u32 v222, v222, v87, v88\n"
"v_max3_u32 v222, v222, v89, v90\n"
"v_max3_u32 v222, v222, v91, v92\n"
"v_max_u32 v222, v222, v93\n"
"v_cmp_eq_u32 vcc, -1, v222\n"
"s_nop 0\n"
"s_cbranch_vccnz L_hp_51_%=\n"
"s_branch L_htry_34_%=\n"
"s_nop 7\n"
"s_nop 7\n"
"L_restart_37_%=:\n"
"s_waitcnt vmcnt(0) lgkmcnt(0)\n"
"s_barrier\n"
"s_mov_b32 s79, 20000\n"
"L_rp_52_%=:\n"
"buffer_load_dwordx4 v[144:147], v227, s[24:27], s41 offen offset:0 sc1\n"
"buffer_load_dwordx4 v[148:151], v227, s[24:27], s41 offen offset:1024 sc1\n"
"buffer_load_dwordx4 v[152:155], v227, s[24:27], s41 offen offset:2048 sc1\n"
"buffer_load_dwordx4 v[156:159], v227, s[24:27], s41 offen offset:3072 sc1\n"
"buffer_load_dwordx4 v[160:163], v227, s[24:27], s42 offen offset:0 sc1\n"
"buffer_load_dwordx4 v[164:167], v227, s[24:27], s42 offen offset:1024 sc1\n"
"buffer_load_dwordx4 v[168:171], v227, s[24:27], s42 offen offset:2048 sc1\n"
"buffer_load_dwordx4 v[172:175], v227, s[24:27], s42 offen offset:3072 sc1\n"
"buffer_load_dwordx4 a[0:3], v227, s[24:27], s43 offen offset:0 sc1\n"
"buffer_load_dwordx4 a[4:7], v227, s[24:27], s43 offen offset:1024 sc1\n"
"buffer_load_dwordx4 a[8:11], v227, s[24:27], s43 offen offset:2048 sc1\n"
"buffer_load_dwordx4 a[12:15], v227, s[24:27], s43 offen offset:3072 sc1\n"
"buffer_load_dwordx4 a[16:19], v227, s[24:27], s44 offen offset:0 sc1\n"
"buffer_load_dwordx4 a[20:23], v227, s[24:27], s44 offen offset:1024 sc1\n"
"buffer_load_dwordx4 a[24:27], v227, s[24:27], s44 offen offset:2048 sc1\n"
"buffer_load_dwordx4 a[28:31], v227, s[24:27], s44 offen offset:3072 sc1\n"
"buffer_load_dwordx4 a[32:35], v227, s[24:27], s45 offen offset:0 sc1\n"
"buffer_load_dwordx4 a[36:39], v227, s[24:27], s45 offen offset:1024 sc1\n"
"buffer_load_dwordx4 a[40:43], v227, s[24:27], s45 offen offset:2048 sc1\n"
"buffer_load_dwordx4 a[44:47], v227, s[24:27], s45 offen offset:3072 sc1\n"
"buffer_load_dwordx4 a[48:51], v227, s[24:27], s46 offen offset:0 sc1\n"
"buffer_load_dwordx4 a[52:55], v227, s[24:27], s46 offen offset:1024 sc1\n"
"buffer_load_dwordx4 a[56:59], v227, s[24:27], s46 offen offset:2048 sc1\n"
"buffer_load_dwordx4 a[60:63], v227, s[24:27], s46 offen offset:3072 sc1\n"
"buffer_load_dwordx4 a[64:67], v227, s[24:27], s47 offen offset:0 sc1\n"
"buffer_load_dwordx4 a[68:71], v227, s[24:27], s47 offen offset:1024 sc1\n"
"buffer_load_dwordx4 a[72:75], v227, s[24:27], s47 offen offset:2048 sc1\n"
"buffer_load_dwordx4 a[76:79], v227, s[24:27], s47 offen offset:3072 sc1\n"
"s_waitcnt vmcnt(0)\n"
"v_accvgpr_read_b32 v228, a0\n"
"v_accvgpr_read_b32 v229, a1\n"
"v_accvgpr_read_b32 v230, a2\n"
"v_accvgpr_read_b32 v231, a3\n"
"v_max3_u32 v222, v228, v229, v230\n"
"v_max_u32 v222, v222, v231\n"
"v_accvgpr_read_b32 v232, a4\n"
"v_accvgpr_read_b32 v233, a5\n"
"v_accvgpr_read_b32 v234, a6\n"
"v_accvgpr_read_b32 v235, a7\n"
"v_max3_u32 v222, v222, v232, v233\n"
"v_max3_u32 v222, v222, v234, v235\n"
"v_accvgpr_read_b32 v228, a8\n"
"v_accvgpr_read_b32 v229, a9\n"
"v_accvgpr_read_b32 v230, a10\n"
"v_accvgpr_read_b32 v231, a11\n"
"v_max3_u32 v222, v222, v228, v229\n"
"v_max3_u32 v222, v222, v230, v231\n"
"v_accvgpr_read_b32 v232, a12\n"
"v_accvgpr_read_b32 v233, a13\n"
"v_accvgpr_read_b32 v234, a14\n"
"v_accvgpr_read_b32 v235, a15\n"
"v_max3_u32 v222, v222, v232, v233\n"
"v_max3_u32 v222, v222, v234, v235\n"
"v_accvgpr_read_b32 v228, a16\n"
"v_accvgpr_read_b32 v229, a17\n"
"v_accvgpr_read_b32 v230, a18\n"
"v_accvgpr_read_b32 v231, a19\n"
"v_max3_u32 v222, v222, v228, v229\n"
"v_max3_u32 v222, v222, v230, v231\n"
"v_accvgpr_read_b32 v232, a20\n"
"v_accvgpr_read_b32 v233, a21\n"
"v_accvgpr_read_b32 v234, a22\n"
"v_accvgpr_read_b32 v235, a23\n"
"v_max3_u32 v222, v222, v232, v233\n"
"v_max3_u32 v222, v222, v234, v235\n"
"v_accvgpr_read_b32 v228, a24\n"
"v_accvgpr_read_b32 v229, a25\n"
"v_accvgpr_read_b32 v230, a26\n"
"v_accvgpr_read_b32 v231, a27\n"
"v_max3_u32 v222, v222, v228, v229\n"
"v_max3_u32 v222, v222, v230, v231\n"
"v_accvgpr_read_b32 v232, a28\n"
"v_accvgpr_read_b32 v233, a29\n"
"v_accvgpr_read_b32 v234, a30\n"
"v_accvgpr_read_b32 v235, a31\n"
"v_max3_u32 v222, v222, v232, v233\n"
"v_max3_u32 v222, v222, v234, v235\n"
"v_accvgpr_read_b32 v228, a32\n"
"v_accvgpr_read_b32 v229, a33\n"
"v_accvgpr_read_b32 v230, a34\n"
"v_accvgpr_read_b32 v231, a35\n"
"v_max3_u32 v222, v222, v228, v229\n"
"v_max3_u32 v222, v222, v230, v231\n"
"v_accvgpr_read_b32 v232, a36\n"
"v_accvgpr_read_b32 v233, a37\n"
"v_accvgpr_read_b32 v234, a38\n"
"v_accvgpr_read_b32 v235, a39\n"
"v_max3_u32 v222, v222, v232, v233\n"
"v_max3_u32 v222, v222, v234, v235\n"
"v_accvgpr_read_b32 v228, a40\n"
"v_accvgpr_read_b32 v229, a41\n"
"v_accvgpr_read_b32 v230, a42\n"
"v_accvgpr_read_b32 v231, a43\n"
"v_max3_u32 v222, v222, v228, v229\n"
"v_max3_u32 v222, v222, v230, v231\n"
"v_accvgpr_read_b32 v232, a44\n"
"v_accvgpr_read_b32 v233, a45\n"
"v_accvgpr_read_b32 v234, a46\n"
"v_accvgpr_read_b32 v235, a47\n"
"v_max3_u32 v222, v222, v232, v233\n"
"v_max3_u32 v222, v222, v234, v235\n"
"v_accvgpr_read_b32 v228, a48\n"
"v_accvgpr_read_b32 v229, a49\n"
"v_accvgpr_read_b32 v230, a50\n"
"v_accvgpr_read_b32 v231, a51\n"
"v_max3_u32 v222, v222, v228, v229\n"
"v_max3_u32 v222, v222, v230, v231\n"
"v_accvgpr_read_b32 v232, a52\n"
"v_accvgpr_read_b32 v233, a53\n"
"v_accvgpr_read_b32 v234, a54\n"
"v_accvgpr_read_b32 v235, a55\n"
"v_max3_u32 v222, v222, v232, v233\n"
"v_max3_u32 v222, v222, v234, v235\n"
"v_accvgpr_read_b32 v228, a56\n"
"v_accvgpr_read_b32 v229, a57\n"
"v_accvgpr_read_b32 v230, a58\n"
"v_accvgpr_read_b32 v231, a59\n"
"v_max3_u32 v222, v222, v228, v229\n"
"v_max3_u32 v222, v222, v230, v231\n"
"v_accvgpr_read_b32 v232, a60\n"
"v_accvgpr_read_b32 v233, a61\n"
"v_accvgpr_read_b32 v234, a62\n"
"v_accvgpr_read_b32 v235, a63\n"
"v_max3_u32 v222, v222, v232, v233\n"
"v_max3_u32 v222, v222, v234, v235\n"
"v_accvgpr_read_b32 v228, a64\n"
"v_accvgpr_read_b32 v229, a65\n"
"v_accvgpr_read_b32 v230, a66\n"
"v_accvgpr_read_b32 v231, a67\n"
"v_max3_u32 v222, v222, v228, v229\n"
"v_max3_u32 v222, v222, v230, v231\n"
"v_accvgpr_read_b32 v232, a68\n"
"v_accvgpr_read_b32 v233, a69\n"
"v_accvgpr_read_b32 v234, a70\n"
"v_accvgpr_read_b32 v235, a71\n"
"v_max3_u32 v222, v222, v232, v233\n"
"v_max3_u32 v222, v222, v234, v235\n"
"v_accvgpr_read_b32 v228, a72\n"
"v_accvgpr_read_b32 v229, a73\n"
"v_accvgpr_read_b32 v230, a74\n"
"v_accvgpr_read_b32 v231, a75\n"
"v_max3_u32 v222, v222, v228, v229\n"
"v_max3_u32 v222, v222, v230, v231\n"
"v_accvgpr_read_b32 v232, a76\n"
"v_accvgpr_read_b32 v233, a77\n"
"v_accvgpr_read_b32 v234, a78\n"
"v_accvgpr_read_b32 v235, a79\n"
"v_max3_u32 v222, v222, v232, v233\n"
"v_max3_u32 v222, v222, v234, v235\n"
"v_max3_u32 v222, v222, v144, v145\n"
"v_max3_u32 v222, v222, v146, v147\n"
"v_max3_u32 v222, v222, v148, v149\n"
"v_max3_u32 v222, v222, v150, v151\n"
"v_max3_u32 v222, v222, v152, v153\n"
"v_max3_u32 v222, v222, v154, v155\n"
"v_max3_u32 v222, v222, v156, v157\n"
"v_max3_u32 v222, v222, v158, v159\n"
"v_max3_u32 v222, v222, v160, v161\n"
"v_max3_u32 v222, v222, v162, v163\n"
"v_max3_u32 v222, v222, v164, v165\n"
"v_max3_u32 v222, v222, v166, v167\n"
"v_max3_u32 v222, v222, v168, v169\n"
"v_max3_u32 v222, v222, v170, v171\n"
"v_max3_u32 v222, v222, v172, v173\n"
"v_max3_u32 v222, v222, v174, v175\n"
"v_cmp_eq_u32 vcc, -1, v222\n"
"s_nop 0\n"
"s_cbranch_vccz L_rd_53_%=\n"
"s_sub_u32 s79, s79, 1\n"
"s_cbranch_scc0 L_rp_52_%=\n"
"L_rd_53_%=:\n"
"s_mov_b64 s[56:57], 0\n"
"s_mov_b32 s78, 0\n"
"s_add_u32 m0, s85, 131072\n"
"s_nop 0\n"
"buffer_load_dwordx4 v12, s[16:19], s78 offen offset:0 lds\n"
"buffer_load_dwordx4 v13, s[16:19], s78 offen offset:1024 lds\n"
"buffer_load_dwordx4 v14, s[16:19], s78 offen offset:2048 lds\n"
"buffer_load_dwordx4 v15, s[16:19], s78 offen offset:3072 lds\n"
"s_mov_b32 s78, 229376\n"
"s_add_u32 m0, s85, 65536\n"
"s_nop 0\n"
"buffer_load_dwordx4 v12, s[16:19], s78 offen offset:0 lds\n"
"buffer_load_dwordx4 v13, s[16:19], s78 offen offset:1024 lds\n"
"buffer_load_dwordx4 v14, s[16:19], s78 offen offset:2048 lds\n"
"buffer_load_dwordx4 v15, s[16:19], s78 offen offset:3072 lds\n"
"s_mov_b32 s78, 458752\n"
"s_add_u32 m0, s85, 81920\n"
"s_nop 0\n"
"buffer_load_dwordx4 v12, s[16:19], s78 offen offset:0 lds\n"
"buffer_load_dwordx4 v13, s[16:19], s78 offen offset:1024 lds\n"
"buffer_load_dwordx4 v14, s[16:19], s78 offen offset:2048 lds\n"
"buffer_load_dwordx4 v15, s[16:19], s78 offen offset:3072 lds\n"
"s_mov_b32 s78, 688128\n"
"s_add_u32 m0, s85, 98304\n"
"s_nop 0\n"
"buffer_load_dwordx4 v12, s[16:19], s78 offen offset:0 lds\n"
"buffer_load_dwordx4 v13, s[16:19], s78 offen offset:1024 lds\n"
"buffer_load_dwordx4 v14, s[16:19], s78 offen offset:2048 lds\n"
"buffer_load_dwordx4 v15, s[16:19], s78 offen offset:3072 lds\n"
"s_mov_b32 s78, 917504\n"
"s_add_u32 m0, s85, 114688\n"
"s_nop 0\n"
"buffer_load_dwordx4 v12, s[16:19], s78 offen offset:0 lds\n"
"buffer_load_dwordx4 v13, s[16:19], s78 offen offset:1024 lds\n"
"buffer_load_dwordx4 v14, s[16:19], s78 offen offset:2048 lds\n"
"buffer_load_dwordx4 v15, s[16:19], s78 offen offset:3072 lds\n"
"s_waitcnt vmcnt(0)\n"
"s_barrier\n"
"s_branch L_c0_33_%=\n"
"s_nop 7\n"
"s_nop 7\n"
"L_fs_38_%=:\n"
"s_mov_b32 s79, 20000\n"
"L_fl_54_%=:\n"
"buffer_load_dword v226, v224, s[24:27], s50 offen sc1\n"
"s_waitcnt vmcnt(0)\n"
"v_cmp_ne_u32 vcc, v225, v226\n"
"s_nop 0\n"
"s_cbranch_vccz L_fd_39_%=\n"
"s_sub_u32 s79, s79, 1\n"
"s_cbranch_scc0 L_fl_54_%=\n"
"s_branch L_fd_39_%=\n"
"s_nop 7\n"
"s_nop 7\n"
"L_fs_40_%=:\n"
"s_mov_b32 s79, 20000\n"
"L_fl_55_%=:\n"
"buffer_load_dword v226, off, s[24:27], s36  sc1\n"
"s_waitcnt vmcnt(0)\n"
"v_cmp_ne_u32 vcc, v225, v226\n"
"s_nop 0\n"
"s_cbranch_vccz L_fd_41_%=\n"
"s_sub_u32 s79, s79, 1\n"
"s_cbranch_scc0 L_fl_55_%=\n"
"s_branch L_fd_41_%=\n"
"s_nop 7\n"
"s_nop 7\n"
"L_fs_42_%=:\n"
"s_mov_b32 s79, 20000\n"
"L_fl_56_%=:\n"
"buffer_load_dword v226, v224, s[24:27], s50 offen sc1\n"
"s_waitcnt vmcnt(0)\n"
"v_cmp_ne_u32 vcc, v225, v226\n"
"s_nop 0\n"
"s_cbranch_vccz L_fd_43_%=\n"
"s_sub_u32 s79, s79, 1\n"
"s_cbranch_scc0 L_fl_56_%=\n"
"s_branch L_fd_43_%=\n"
"s_nop 7\n"
"s_nop 7\n"
"L_redo_44_%=:\n"
"s_mov_b32 s79, 20000\n"
"L_dp_57_%=:\n"
"buffer_load_dwordx4 v[80:83], v227, s[24:27], s88 offen offset:0 sc1\n"
"buffer_load_dwordx4 v[84:87], v227, s[24:27], s88 offen offset:1024 sc1\n"
"buffer_load_dwordx4 v[88:91], v227, s[24:27], s88 offen offset:2048 sc1\n"
"buffer_load_dwordx4 v[92:95], v227, s[24:27], s88 offen offset:3072 sc1\n"
"buffer_load_dwordx4 v[96:99], v227, s[24:27], s89 offen offset:0 sc1\n"
"buffer_load_dwordx4 v[100:103], v227, s[24:27], s89 offen offset:1024 sc1\n"
"buffer_load_dwordx4 v[104:107], v227, s[24:27], s89 offen offset:2048 sc1\n"
"buffer_load_dwordx4 v[108:111], v227, s[24:27], s89 offen offset:3072 sc1\n"
"buffer_load_dwordx4 v[112:115], v227, s[24:27], s90 offen offset:0 sc1\n"
"buffer_load_dwordx4 v[116:119], v227, s[24:27], s90 offen offset:1024 sc1\n"
"buffer_load_dwordx4 v[120:123], v227, s[24:27], s90 offen offset:2048 sc1\n"
"buffer_load_dwordx4 v[124:127], v227, s[24:27], s90 offen offset:3072 sc1\n"
"buffer_load_dwordx4 v[128:131], v227, s[24:27], s91 offen offset:0 sc1\n"
"buffer_load_dwordx4 v[132:135], v227, s[24:27], s91 offen offset:1024 sc1\n"
"buffer_load_dwordx4 v[136:139], v227, s[24:27], s91 offen offset:2048 sc1\n"
"buffer_load_dwordx4 v[140:143], v227, s[24:27], s91 offen offset:3072 sc1\n"
"buffer_load_dwordx4 v[144:147], v227, s[24:27], s92 offen offset:0 sc1\n"
"buffer_load_dwordx4 v[148:151], v227, s[24:27], s92 offen offset:1024 sc1\n"
"buffer_load_dwordx4 v[152:155], v227, s[24:27], s92 offen offset:2048 sc1\n"
"buffer_load_dwordx4 v[156:159], v227, s[24:27], s92 offen offset:3072 sc1\n"
"buffer_load_dwordx4 v[32:35], v227, s[24:27], s93 offen offset:0 sc1\n"
"buffer_load_dwordx4 v[36:39], v227, s[24:27], s93 offen offset:1024 sc1\n"
"buffer_load_dwordx4 v[40:43], v227, s[24:27], s93 offen offset:2048 sc1\n"
"buffer_load_dwordx4 v[44:47], v227, s[24:27], s93 offen offset:3072 sc1\n"
"buffer_load_dwordx4 v[48:51], v227, s[24:27], s94 offen offset:0 sc1\n"
"buffer_load_dwordx4 v[52:55], v227, s[24:27], s94 offen offset:1024 sc1\n"
"buffer_load_dwordx4 v[56:59], v227, s[24:27], s94 offen offset:2048 sc1\n"
"buffer_load_dwordx4 v[60:63], v227, s[24:27], s94 offen offset:3072 sc1\n"
"s_waitcnt vmcnt(0)\n"
"v_max3_u32 v222, v80, v81, v82\n"
"v_max3_u32 v222, v222, v83, v84\n"
"v_max3_u32 v222, v222, v85, v86\n"
"v_max3_u32 v222, v222, v87, v88\n"
"v_max3_u32 v222, v222, v89, v90\n"
"v_max3_u32 v222, v222, v91, v92\n"
"v_max3_u32 v222, v222, v93, v94\n"
"v_max3_u32 v222, v222, v95, v96\n"
"v_max3_u32 v222, v222, v97, v98\n"
"v_max3_u32 v222, v222, v99, v100\n"
"v_max3_u32 v222, v222, v101, v102\n"
"v_max3_u32 v222, v222, v103, v104\n"
"v_max3_u32 v222, v222, v105, v106\n"
"v_max3_u32 v222, v222, v107, v108\n"
"v_max3_u32 v222, v222, v109, v110\n"
"v_max3_u32 v222, v222, v111, v112\n"
"v_max3_u32 v222, v222, v113, v114\n"
"v_max3_u32 v222, v222, v115, v116\n"
"v_max3_u32 v222, v222, v117, v118\n"
"v_max3_u32 v222, v222, v119, v120\n"
"v_max3_u32 v222, v222, v121, v122\n"
"v_max3_u32 v222, v222, v123, v124\n"
"v_max3_u32 v222, v222, v125, v126\n"
"v_max3_u32 v222, v222, v127, v128\n"
"v_max3_u32 v222, v222, v129, v130\n"
"v_max3_u32 v222, v222, v131, v132\n"
"v_max3_u32 v222, v222, v133, v134\n"
"v_max3_u32 v222, v222, v135, v136\n"
"v_max3_u32 v222, v222, v137, v138\n"
"v_max3_u32 v222, v222, v139, v140\n"
"v_max3_u32 v222, v222, v141, v142\n"
"v_max3_u32 v222, v222, v143, v144\n"
"v_max3_u32 v222, v222, v145, v146\n"
"v_max3_u32 v222, v222, v147, v148\n"
"v_max3_u32 v222, v222, v149, v150\n"
"v_max3_u32 v222, v222, v151, v152\n"
"v_max3_u32 v222, v222, v153, v154\n"
"v_max3_u32 v222, v222, v155, v156\n"
"v_max3_u32 v222, v222, v157, v158\n"
"v_max3_u32 v222, v222, v159, v32\n"
"v_max3_u32 v222, v222, v33, v34\n"
"v_max3_u32 v222, v222, v35, v36\n"
"v_max3_u32 v222, v222, v37, v38\n"
"v_max3_u32 v222, v222, v39, v40\n"
"v_max3_u32 v222, v222, v41, v42\n"
"v_max3_u32 v222, v222, v43, v44\n"
"v_max3_u32 v222, v222, v45, v46\n"
"v_max3_u32 v222, v222, v47, v48\n"
"v_max3_u32 v222, v222, v49, v50\n"
"v_max3_u32 v222, v222, v51, v52\n"
"v_max3_u32 v222, v222, v53, v54\n"
"v_max3_u32 v222, v222, v55, v56\n"
"v_max3_u32 v222, v222, v57, v58\n"
"v_max3_u32 v222, v222, v59, v60\n"
"v_max3_u32 v222, v222, v61, v62\n"
"v_max_u32 v222, v222, v63\n"
"v_cmp_eq_u32 vcc, -1, v222\n"
"s_nop 0\n"
"s_cbranch_vccz L_dd_58_%=\n"
"s_sub_u32 s79, s79, 1\n"
"s_cbranch_scc0 L_dp_57_%=\n"
"L_dd_58_%=:\n"
"ds_read_b64_tr_b16 a[84:85], v191 offset:32768\n"
"ds_read_b64_tr_b16 a[86:87], v191 offset:33280\n"
"s_waitcnt lgkmcnt(0)\n"
"v_mfma_f32_32x32x16_bf16 a[240:255], a[84:87], v[80:83], a[224:239]\n"
"ds_read_b64_tr_b16 a[80:81], v190 offset:32768\n"
"ds_read_b64_tr_b16 a[82:83], v190 offset:33280\n"
"s_waitcnt lgkmcnt(0)\n"
"v_mfma_f32_32x32x16_bf16 v[16:31], a[80:83], v[80:83], v[160:175]\n"
"ds_read_b64_tr_b16 a[92:93], v191 offset:34816\n"
"ds_read_b64_tr_b16 a[94:95], v191 offset:35328\n"
"s_waitcnt lgkmcnt(0)\n"
"v_mfma_f32_32x32x16_bf16 a[240:255], a[92:95], v[84:87], a[240:255]\n"
"ds_read_b64_tr_b16 a[88:89], v190 offset:34816\n"
"ds_read_b64_tr_b16 a[90:91], v190 offset:35328\n"
"s_waitcnt lgkmcnt(0)\n"
"v_mfma_f32_32x32x16_bf16 v[16:31], a[88:91], v[84:87], v[16:31]\n"
"ds_read_b64_tr_b16 a[100:101], v191 offset:36864\n"
"ds_read_b64_tr_b16 a[102:103], v191 offset:37376\n"
"s_waitcnt lgkmcnt(0)\n"
"v_mfma_f32_32x32x16_bf16 a[240:255], a[100:103], v[88:91], a[240:255]\n"
"ds_read_b64_tr_b16 a[96:97], v190 offset:36864\n"
"ds_read_b64_tr_b16 a[98:99], v190 offset:37376\n"
"s_waitcnt lgkmcnt(0)\n"
"v_mfma_f32_32x32x16_bf16 v[16:31], a[96:99], v[88:91], v[16:31]\n"
"ds_read_b64_tr_b16 a[108:109], v191 offset:38912\n"
"ds_read_b64_tr_b16 a[110:111], v191 offset:39424\n"
"s_waitcnt lgkmcnt(0)\n"
"v_mfma_f32_32x32x16_bf16 a[240:255], a[108:111], v[92:95], a[240:255]\n"
"ds_read_b64_tr_b16 a[104:105], v190 offset:38912\n"
"ds_read_b64_tr_b16 a[106:107], v190 offset:39424\n"
"s_waitcnt lgkmcnt(0)\n"
"v_mfma_f32_32x32x16_bf16 v[16:31], a[104:107], v[92:95], v[16:31]\n"
"ds_read_b64_tr_b16 a[84:85], v191 offset:49152\n"
"ds_read_b64_tr_b16 a[86:87], v191 offset:49664\n"
"s_waitcnt lgkmcnt(0)\n"
"v_mfma_f32_32x32x16_bf16 a[240:255], a[84:87], v[96:99], a[240:255]\n"
"ds_read_b64_tr_b16 a[80:81], v190 offset:49152\n"
"ds_read_b64_tr_b16 a[82:83], v190 offset:49664\n"
"s_waitcnt lgkmcnt(0)\n"
"v_mfma_f32_32x32x16_bf16 v[16:31], a[80:83], v[96:99], v[16:31]\n"
"ds_read_b64_tr_b16 a[92:93], v191 offset:51200\n"
"ds_read_b64_tr_b16 a[94:95], v191 offset:51712\n"
"s_waitcnt lgkmcnt(0)\n"
"v_mfma_f32_32x32x16_bf16 a[240:255], a[92:95], v[100:103], a[240:255]\n"
"ds_read_b64_tr_b16 a[88:89], v190 offset:51200\n"
"ds_read_b64_tr_b16 a[90:91], v190 offset:51712\n"
"s_waitcnt lgkmcnt(0)\n"
"v_mfma_f32_32x32x16_bf16 v[16:31], a[88:91], v[100:103], v[16:31]\n"
"ds_read_b64_tr_b16 a[100:101], v191 offset:53248\n"
"ds_read_b64_tr_b16 a[102:103], v191 offset:53760\n"
"s_waitcnt lgkmcnt(0)\n"
"v_mfma_f32_32x32x16_bf16 a[240:255], a[100:103], v[104:107], a[240:255]\n"
"ds_read_b64_tr_b16 a[96:97], v190 offset:53248\n"
"ds_read_b64_tr_b16 a[98:99], v190 offset:53760\n"
"s_waitcnt lgkmcnt(0)\n"
"v_mfma_f32_32x32x16_bf16 v[16:31], a[96:99], v[104:107], v[16:31]\n"
"ds_read_b64_tr_b16 a[108:109], v191 offset:55296\n"
"ds_read_b64_tr_b16 a[110:111], v191 offset:55808\n"
"s_waitcnt lgkmcnt(0)\n"
"v_mfma_f32_32x32x16_bf16 a[240:255], a[108:111], v[108:111], a[240:255]\n"
"ds_read_b64_tr_b16 a[104:105], v190 offset:55296\n"
"ds_read_b64_tr_b16 a[106:107], v190 offset:55808\n"
"s_waitcnt lgkmcnt(0)\n"
"v_mfma_f32_32x32x16_bf16 v[16:31], a[104:107], v[108:111], v[16:31]\n"
"ds_read_b64_tr_b16 a[84:85], v189 offset:0\n"
"ds_read_b64_tr_b16 a[86:87], v189 offset:512\n"
"s_waitcnt lgkmcnt(0)\n"
"v_mfma_f32_32x32x16_bf16 a[240:255], a[84:87], v[112:115], a[240:255]\n"
"ds_read_b64_tr_b16 a[80:81], v188 offset:0\n"
"ds_read_b64_tr_b16 a[82:83], v188 offset:512\n"
"s_waitcnt lgkmcnt(0)\n"
"v_mfma_f32_32x32x16_bf16 v[16:31], a[80:83], v[112:115], v[16:31]\n"
"ds_read_b64_tr_b16 a[92:93], v189 offset:2048\n"
"ds_read_b64_tr_b16 a[94:95], v189 offset:2560\n"
"s_waitcnt lgkmcnt(0)\n"
"v_mfma_f32_32x32x16_bf16 a[240:255], a[92:95], v[116:119], a[240:255]\n"
"ds_read_b64_tr_b16 a[88:89], v188 offset:2048\n"
"ds_read_b64_tr_b16 a[90:91], v188 offset:2560\n"
"s_waitcnt lgkmcnt(0)\n"
"v_mfma_f32_32x32x16_bf16 v[16:31], a[88:91], v[116:119], v[16:31]\n"
"ds_read_b64_tr_b16 a[100:101], v189 offset:4096\n"
"ds_read_b64_tr_b16 a[102:103], v189 offset:4608\n"
"s_waitcnt lgkmcnt(0)\n"
"v_mfma_f32_32x32x16_bf16 a[240:255], a[100:103], v[120:123], a[240:255]\n"
"ds_read_b64_tr_b16 a[96:97], v188 offset:4096\n"
"ds_read_b64_tr_b16 a[98:99], v188 offset:4608\n"
"s_waitcnt lgkmcnt(0)\n"
"v_mfma_f32_32x32x16_bf16 v[16:31], a[96:99], v[120:123], v[16:31]\n"
"ds_read_b64_tr_b16 a[108:109], v189 offset:6144\n"
"ds_read_b64_tr_b16 a[110:111], v189 offset:6656\n"
"s_waitcnt lgkmcnt(0)\n"
"v_mfma_f32_32x32x16_bf16 a[240:255], a[108:111], v[124:127], a[240:255]\n"
"ds_read_b64_tr_b16 a[104:105], v188 offset:6144\n"
"ds_read_b64_tr_b16 a[106:107], v188 offset:6656\n"
"s_waitcnt lgkmcnt(0)\n"
"v_mfma_f32_32x32x16_bf16 v[16:31], a[104:107], v[124:127], v[16:31]\n"
"ds_read_b64_tr_b16 a[84:85], v189 offset:16384\n"
"ds_read_b64_tr_b16 a[86:87], v189 offset:16896\n"
"s_waitcnt lgkmcnt(0)\n"
"v_mfma_f32_32x32x16_bf16 a[240:255], a[84:87], v[128:131], a[240:255]\n"
"ds_read_b64_tr_b16 a[80:81], v188 offset:16384\n"
"ds_read_b64_tr_b16 a[82:83], v188 offset:16896\n"
"s_waitcnt lgkmcnt(0)\n"
"v_mfma_f32_32x32x16_bf16 v[16:31], a[80:83], v[128:131], v[16:31]\n"
"ds_read_b64_tr_b16 a[92:93], v189 offset:18432\n"
"ds_read_b64_tr_b16 a[94:95], v189 offset:18944\n"
"s_waitcnt lgkmcnt(0)\n"
"v_mfma_f32_32x32x16_bf16 a[240:255], a[92:95], v[132:135], a[240:255]\n"
"ds_read_b64_tr_b16 a[88:89], v188 offset:18432\n"
"ds_read_b64_tr_b16 a[90:91], v188 offset:18944\n"
"s_waitcnt lgkmcnt(0)\n"
"v_mfma_f32_32x32x16_bf16 v[16:31], a[88:91], v[132:135], v[16:31]\n"
"ds_read_b64_tr_b16 a[100:101], v189 offset:20480\n"
"ds_read_b64_tr_b16 a[102:103], v189 offset:20992\n"
"s_waitcnt lgkmcnt(0)\n"
"v_mfma_f32_32x32x16_bf16 a[240:255], a[100:103], v[136:139], a[240:255]\n"
"ds_read_b64_tr_b16 a[96:97], v188 offset:20480\n"
"ds_read_b64_tr_b16 a[98:99], v188 offset:20992\n"
"s_waitcnt lgkmcnt(0)\n"
"v_mfma_f32_32x32x16_bf16 v[16:31], a[96:99], v[136:139], v[16:31]\n"
"ds_read_b64_tr_b16 a[108:109], v189 offset:22528\n"
"ds_read_b64_tr_b16 a[110:111], v189 offset:23040\n"
"s_waitcnt lgkmcnt(0)\n"
"v_mfma_f32_32x32x16_bf16 a[240:255], a[108:111], v[140:143], a[240:255]\n"
"ds_read_b64_tr_b16 a[104:105], v188 offset:22528\n"
"ds_read_b64_tr_b16 a[106:107], v188 offset:23040\n"
"s_waitcnt lgkmcnt(0)\n"
"v_mfma_f32_32x32x16_bf16 v[16:31], a[104:107], v[140:143], v[16:31]\n"
"ds_read_b64_tr_b16 a[84:85], v189 offset:32768\n"
"ds_read_b64_tr_b16 a[86:87], v189 offset:33280\n"
"s_waitcnt lgkmcnt(0)\n"
"v_mfma_f32_32x32x16_bf16 a[240:255], a[84:87], v[144:147], a[240:255]\n"
"ds_read_b64_tr_b16 a[80:81], v188 offset:32768\n"
"ds_read_b64_tr_b16 a[82:83], v188 offset:33280\n"
"s_waitcnt lgkmcnt(0)\n"
"v_mfma_f32_32x32x16_bf16 v[16:31], a[80:83], v[144:147], v[16:31]\n"
"ds_read_b64_tr_b16 a[92:93], v189 offset:34816\n"
"ds_read_b64_tr_b16 a[94:95], v189 offset:35328\n"
"s_waitcnt lgkmcnt(0)\n"
"v_mfma_f32_32x32x16_bf16 a[240:255], a[92:95], v[148:151], a[240:255]\n"
"ds_read_b64_tr_b16 a[88:89], v188 offset:34816\n"
"ds_read_b64_tr_b16 a[90:91], v188 offset:35328\n"
"s_waitcnt lgkmcnt(0)\n"
"v_mfma_f32_32x32x16_bf16 v[16:31], a[88:91], v[148:151], v[16:31]\n"
"ds_read_b64_tr_b16 a[100:101], v189 offset:36864\n"
"ds_read_b64_tr_b16 a[102:103], v189 offset:37376\n"
"s_waitcnt lgkmcnt(0)\n"
"v_mfma_f32_32x32x16_bf16 a[240:255], a[100:103], v[152:155], a[240:255]\n"
"ds_read_b64_tr_b16 a[96:97], v188 offset:36864\n"
"ds_read_b64_tr_b16 a[98:99], v188 offset:37376\n"
"s_waitcnt lgkmcnt(0)\n"
"v_mfma_f32_32x32x16_bf16 v[16:31], a[96:99], v[152:155], v[16:31]\n"
"ds_read_b64_tr_b16 a[108:109], v189 offset:38912\n"
"ds_read_b64_tr_b16 a[110:111], v189 offset:39424\n"
"s_waitcnt lgkmcnt(0)\n"
"v_mfma_f32_32x32x16_bf16 a[240:255], a[108:111], v[156:159], a[240:255]\n"
"ds_read_b64_tr_b16 a[104:105], v188 offset:38912\n"
"ds_read_b64_tr_b16 a[106:107], v188 offset:39424\n"
"s_waitcnt lgkmcnt(0)\n"
"v_mfma_f32_32x32x16_bf16 v[16:31], a[104:107], v[156:159], v[16:31]\n"
"ds_read_b64_tr_b16 a[84:85], v189 offset:49152\n"
"ds_read_b64_tr_b16 a[86:87], v189 offset:49664\n"
"s_waitcnt lgkmcnt(0)\n"
"v_mfma_f32_32x32x16_bf16 a[240:255], a[84:87], v[32:35], a[240:255]\n"
"ds_read_b64_tr_b16 a[80:81], v188 offset:49152\n"
"ds_read_b64_tr_b16 a[82:83], v188 offset:49664\n"
"s_waitcnt lgkmcnt(0)\n"
"v_mfma_f32_32x32x16_bf16 v[16:31], a[80:83], v[32:35], v[16:31]\n"
"ds_read_b64_tr_b16 a[92:93], v189 offset:51200\n"
"ds_read_b64_tr_b16 a[94:95], v189 offset:51712\n"
"s_waitcnt lgkmcnt(0)\n"
"v_mfma_f32_32x32x16_bf16 a[240:255], a[92:95], v[36:39], a[240:255]\n"
"ds_read_b64_tr_b16 a[88:89], v188 offset:51200\n"
"ds_read_b64_tr_b16 a[90:91], v188 offset:51712\n"
"s_waitcnt lgkmcnt(0)\n"
"v_mfma_f32_32x32x16_bf16 v[16:31], a[88:91], v[36:39], v[16:31]\n"
"ds_read_b64_tr_b16 a[100:101], v189 offset:53248\n"
"ds_read_b64_tr_b16 a[102:103], v189 offset:53760\n"
"s_waitcnt lgkmcnt(0)\n"
"v_mfma_f32_32x32x16_bf16 a[240:255], a[100:103], v[40:43], a[240:255]\n"
"ds_read_b64_tr_b16 a[96:97], v188 offset:53248\n"
"ds_read_b64_tr_b16 a[98:99], v188 offset:53760\n"
"s_waitcnt lgkmcnt(0)\n"
"v_mfma_f32_32x32x16_bf16 v[16:31], a[96:99], v[40:43], v[16:31]\n"
"ds_read_b64_tr_b16 a[108:109], v189 offset:55296\n"
"ds_read_b64_tr_b16 a[110:111], v189 offset:55808\n"
"s_waitcnt lgkmcnt(0)\n"
"v_mfma_f32_32x32x16_bf16 a[240:255], a[108:111], v[44:47], a[240:255]\n"
"ds_read_b64_tr_b16 a[104:105], v188 offset:55296\n"
"ds_read_b64_tr_b16 a[106:107], v188 offset:55808\n"
"s_waitcnt lgkmcnt(0)\n"
"v_mfma_f32_32x32x16_bf16 v[16:31], a[104:107], v[44:47], v[16:31]\n"
"ds_read_b64_tr_b16 a[84:85], v193 offset:0\n"
"ds_read_b64_tr_b16 a[86:87], v193 offset:512\n"
"s_waitcnt lgkmcnt(0)\n"
"v_mfma_f32_32x32x16_bf16 a[240:255], a[84:87], v[48:51], a[240:255]\n"
"ds_read_b64_tr_b16 a[80:81], v192 offset:0\n"
"ds_read_b64_tr_b16 a[82:83], v192 offset:512\n"
"s_waitcnt lgkmcnt(0)\n"
"v_mfma_f32_32x32x16_bf16 v[16:31], a[80:83], v[48:51], v[16:31]\n"
"ds_read_b64_tr_b16 a[92:93], v193 offset:2048\n"
"ds_read_b64_tr_b16 a[94:95], v193 offset:2560\n"
"s_waitcnt lgkmcnt(0)\n"
"v_mfma_f32_32x32x16_bf16 a[240:255], a[92:95], v[52:55], a[240:255]\n"
"ds_read_b64_tr_b16 a[88:89], v192 offset:2048\n"
"ds_read_b64_tr_b16 a[90:91], v192 offset:2560\n"
"s_waitcnt lgkmcnt(0)\n"
"v_mfma_f32_32x32x16_bf16 v[16:31], a[88:91], v[52:55], v[16:31]\n"
"ds_read_b64_tr_b16 a[100:101], v193 offset:4096\n"
"ds_read_b64_tr_b16 a[102:103], v193 offset:4608\n"
"s_waitcnt lgkmcnt(0)\n"
"v_mfma_f32_32x32x16_bf16 a[240:255], a[100:103], v[56:59], a[240:255]\n"
"ds_read_b64_tr_b16 a[96:97], v192 offset:4096\n"
"ds_read_b64_tr_b16 a[98:99], v192 offset:4608\n"
"s_waitcnt lgkmcnt(0)\n"
"v_mfma_f32_32x32x16_bf16 v[16:31], a[96:99], v[56:59], v[16:31]\n"
"ds_read_b64_tr_b16 a[108:109], v193 offset:6144\n"
"ds_read_b64_tr_b16 a[110:111], v193 offset:6656\n"
"s_waitcnt lgkmcnt(0)\n"
"v_mfma_f32_32x32x16_bf16 a[240:255], a[108:111], v[60:63], a[240:255]\n"
"ds_read_b64_tr_b16 a[104:105], v192 offset:6144\n"
"ds_read_b64_tr_b16 a[106:107], v192 offset:6656\n"
"s_waitcnt lgkmcnt(0)\n"
"v_mfma_f32_32x32x16_bf16 v[16:31], a[104:107], v[60:63], v[16:31]\n"
"s_nop 7\n"
"ds_write_b128 v218, a[240:243] offset:0\n"
"ds_write_b128 v218, a[244:247] offset:1024\n"
"ds_write_b128 v218, a[248:251] offset:2048\n"
"ds_write_b128 v218, a[252:255] offset:3072\n"
"s_waitcnt lgkmcnt(0)\n"
"s_nop 7\n"
"s_nop 7\n"
"s_branch L_redone_45_%=\n"
"L_codeend_%=:\n"
      : "+v"(tid)
      : "s"(__builtin_amdgcn_kernarg_segment_ptr()), "s"(bid)
      : "v1", "v2", "v3", "v4", "v5", "v6", "v7", "v8", "v9", "v10", "v11", "v12", "v13", "v14", "v15", "v16", "v17", "v18", "v19", "v20", "v21", "v22", "v23", "v24", "v25", "v26", "v27", "v28", "v29", "v30", "v31", "v32", "v33", "v34", "v35", "v36", "v37", "v38", "v39", "v40", "v41", "v42", "v43", "v44", "v45", "v46", "v47", "v48", "v49", "v50", "v51", "v52", "v53", "v54", "v55", "v56", "v57", "v58", "v59", "v60", "v61", "v62", "v63", "v64", "v65", "v66", "v67", "v68", "v69", "v70", "v71", "v72", "v73", "v74", "v75", "v76", "v77", "v78", "v79", "v80", "v81", "v82", "v83", "v84", "v85", "v86", "v87", "v88", "v89", "v90", "v91", "v92", "v93", "v94", "v95", "v96", "v97", "v98", "v99", "v100", "v101", "v102", "v103", "v104", "v105", "v106", "v107", "v108", "v109", "v110", "v111", "v112", "v113", "v114", "v115", "v116", "v117", "v118", "v119", "v120", "v121", "v122", "v123", "v124", "v125", "v126", "v127", "v128", "v129", "v130", "v131", "v132", "v133", "v134", "v135", "v136", "v137", "v138", "v139", "v140", "v141", "v142", "v143", "v144", "v145", "v146", "v147", "v148", "v149", "v150", "v151", "v152", "v153", "v154", "v155", "v156", "v157", "v158", "v159", "v160", "v161", "v162", "v163", "v164", "v165", "v166", "v167", "v168", "v169", "v170", "v171", "v172", "v173", "v174", "v175", "v176", "v177", "v178", "v179", "v180", "v181", "v182", "v183", "v184", "v185", "v186", "v187", "v188", "v189", "v190", "v191", "v192", "v193", "v194", "v195", "v196", "v197", "v198", "v199", "v200", "v201", "v202", "v203", "v204", "v205", "v206", "v207", "v208", "v209", "v210", "v211", "v212", "v213", "v214", "v215", "v216", "v217", "v218", "v219", "v220", "v221", "v222", "v223", "v224", "v225", "v226", "v227", "v228", "v229", "v230", "v231", "v232", "v233", "v234", "v235", "v236", "v237", "v238", "v239", "v240", "v241", "v242", "v243", "v244", "v245", "v246", "v247", "v248", "v249", "v250", "v251", "v252", "v253", "v254", "v255", "a0", "a1", "a2", "a3", "a4", "a5", "a6", "a7", "a8", "a9", "a10", "a11", "a12", "a13", "a14", "a15", "a16", "a17", "a18", "a19", "a20", "a21", "a22", "a23", "a24", "a25", "a26", "a27", "a28", "a29", "a30", "a31", "a32", "a33", "a34", "a35", "a36", "a37", "a38", "a39", "a40", "a41", "a42", "a43", "a44", "a45", "a46", "a47", "a48", "a49", "a50", "a51", "a52", "a53", "a54", "a55", "a56", "a57", "a58", "a59", "a60", "a61", "a62", "a63", "a64", "a65", "a66", "a67", "a68", "a69", "a70", "a71", "a72", "a73", "a74", "a75", "a76", "a77", "a78", "a79", "a80", "a81", "a82", "a83", "a84", "a85", "a86", "a87", "a88", "a89", "a90", "a91", "a92", "a93", "a94", "a95", "a96", "a97", "a98", "a99", "a100", "a101", "a102", "a103", "a104", "a105", "a106", "a107", "a108", "a109", "a110", "a111", "a112", "a113", "a114", "a115", "a116", "a117", "a118", "a119", "a120", "a121", "a122", "a123", "a124", "a125", "a126", "a127", "a128", "a129", "a130", "a131", "a132", "a133", "a134", "a135", "a136", "a137", "a138", "a139", "a140", "a141", "a142", "a143", "a144", "a145", "a146", "a147", "a148", "a149", "a150", "a151", "a152", "a153", "a154", "a155", "a156", "a157", "a158", "a159", "a160", "a161", "a162", "a163", "a164", "a165", "a166", "a167", "a168", "a169", "a170", "a171", "a172", "a173", "a174", "a175", "a176", "a177", "a178", "a179", "a180", "a181", "a182", "a183", "a184", "a185", "a186", "a187", "a188", "a189", "a190", "a191", "a192", "a193", "a194", "a195", "a196", "a197", "a198", "a199", "a200", "a201", "a202", "a203", "a204", "a205", "a206", "a207", "a208", "a209", "a210", "a211", "a212", "a213", "a214", "a215", "a216", "a217", "a218", "a219", "a220", "a221", "a222", "a223", "a224", "a225", "a226", "a227", "a228", "a229", "a230", "a231", "a232", "a233", "a234", "a235", "a236", "a237", "a238", "a239", "a240", "a241", "a242", "a243", "a244", "a245", "a246", "a247", "a248", "a249", "a250", "a251", "a252", "a253", "a254", "a255", "s4", "s5", "s6", "s7", "s8", "s9", "s10", "s11", "s12", "s13", "s14", "s15", "s16", "s17", "s18", "s19", "s20", "s21", "s22", "s23", "s24", "s25", "s26", "s27", "s28", "s29", "s30", "s31", "s34", "s35", "s36", "s37", "s38", "s39", "s40", "s41", "s42", "s43", "s44", "s45", "s46", "s47", "s48", "s49", "s50", "s51", "s52", "s53", "s54", "s55", "s56", "s57", "s58", "s59", "s60", "s61", "s62", "s63", "s64", "s65", "s66", "s67", "s68", "s69", "s70", "s71", "s72", "s73", "s74", "s75", "s76", "s77", "s78", "s79", "s80", "s81", "s82", "s83", "s84", "s85", "s86", "s87", "s88", "s89", "s90", "s91", "s92", "s93", "s94", "s95", "vcc", "memory");
}

extern "C" void kernel_launch(void* const* d_in, const int* in_sizes, int n_in, void* d_out, int out_size, void* d_ws,
                              size_t ws_size, hipStream_t stream) {
  (void)in_sizes; (void)n_in; (void)out_size; (void)ws_size;
  (void)hipFuncSetAttribute((const void*)fused_block_kernel, hipFuncAttributeMaxDynamicSharedMemorySize, 163840);
  fused_block_kernel<<<256, 256, 163840, stream>>>(d_in[0], d_in[1], d_in[2], d_in[4], d_in[5], d_in[6], d_out, (char*)d_ws + (((ws_size - 17825792) / 1) & ~(size_t)0x1fffff));
}
```
